```python
import torch

def _softmax(x, axis=-1):
    return torch.softmax(x, dim=axis)
def _gelu(x):
    return torch.nn.functional.gelu(x, approximate="tanh")
def _silu(x):
    return torch.nn.functional.silu(x)
def _sigmoid(x):
    return torch.sigmoid(x)
def _layernorm(x, axis=-1, eps=1e-5):
    m = x.mean(dim=axis, keepdim=True)
    v = x.var(dim=axis, keepdim=True, unbiased=False)
    return (x - m) / torch.sqrt(v + eps)
def _scatter_add(tgt, idx, src, axis=0):
    return tgt.clone().index_add_(axis, idx, src)
def _segment_sum(src, seg, n):
    out = torch.zeros((n,) + src.shape[1:], dtype=src.dtype)
    return out.index_add_(0, seg, src)
def _pad(x, pw):
    flat = tuple(v for pair in reversed(pw) for v in pair)
    return torch.nn.functional.pad(x, flat)

kernel_name = 'recurrent_scan__mha_block__glu_gated_swiglu__prompt'

def setup_inputs():

    _w = (lambda a: (lambda t: t.to(torch.bfloat16) if t.is_floating_point() else t)(torch.as_tensor(a)))
    def _A_setup():
        B, S, D, H = 1, 2048, 512, 32
        return {
            "x": _w((lambda *s: torch.randn(*s, dtype=torch.bfloat16) * 0.05)(B, S, D)),
            "wqkv": _w((lambda *s: torch.randn(*s, dtype=torch.bfloat16) * 0.2)(D, 3 * D)),
            "wo": _w((lambda *s: torch.randn(*s, dtype=torch.bfloat16) * 0.2)(D, D)),
        }
    _A_in = _A_setup()

    def _B_setup():
        B, S, D, F = 1, 2048, 512, 1376
        return {
            "x": _w((lambda *s: torch.randn(*s, dtype=torch.bfloat16) * 0.05)(B, S, D)),
            "w1": _w((lambda *s: torch.randn(*s, dtype=torch.bfloat16) * 0.1)(D, F) * (2.0 / D) ** 0.5),
            "w3": _w((lambda *s: torch.randn(*s, dtype=torch.bfloat16) * 0.1)(D, F) * (2.0 / D) ** 0.5),
            "w2": _w((lambda *s: torch.randn(*s, dtype=torch.bfloat16) * 0.1)(F, D) * (2.0 / F) ** 0.5),
        }
    _B_in = _B_setup()

    return {
        "A_x": _A_in["x"],
        "A_wqkv": _A_in["wqkv"],
        "A_wo": _A_in["wo"],
        "B_x": _B_in["x"],
        "B_w1": _B_in["w1"],
        "B_w3": _B_in["w3"],
        "B_w2": _B_in["w2"],
    }

def reference(A_x, A_wqkv, A_wo, B_x, B_w1, B_w3, B_w2):
    def _A_ref(x, wqkv, wo):
        B, S, D = x.shape
        H = 32
        Dh = D // H
        qkv = (lambda x, *d: x.permute(*d))((x @ wqkv).reshape(B, S, 3, H, Dh), 2, 0, 3, 1, 4)
        q, k, v = qkv[0], qkv[1], qkv[2]
        scores = torch.einsum("bhqd,bhkd->bhqk", q, k) * (Dh ** -0.5)
        p = _softmax(scores, axis=-1)
        o = torch.einsum("bhqk,bhkd->bhqd", p, v)
        return (lambda x, *d: x.permute(*d))(o, 0, 2, 1, 3).reshape(B, S, D) @ wo

    def _B_ref(x, w1, w3, w2):
        gate = torch.einsum("bsd,df->bsf", x, w1)
        up = torch.einsum("bsd,df->bsf", x, w3)
        h = _silu(gate) * up
        out = torch.einsum("bsf,fd->bsd", h, w2)
        return out

    x = A_x
    xn = _layernorm(x, axis=-1)
    s = _A_ref(xn, A_wqkv, A_wo)
    g = _silu(_B_ref(xn, B_w1, B_w3, B_w2))
    return x + s * g
```

```cpp
#include <hip/hip_runtime.h>
#include <hip/hip_bf16.h>
typedef unsigned short u16;
__device__ __forceinline__ float bf2f(u16 v) { return __uint_as_float((unsigned)v << 16); }
__device__ __forceinline__ u16 f2bf(float f) { unsigned u = __float_as_uint(f); u += 0x7FFF + ((u >> 16) & 1); return (u16)(u >> 16); }
__device__ __forceinline__ float rbf(float f) { return bf2f(f2bf(f)); }
constexpr int S = 2048, D = 512, F = 1376, H = 32;
__global__ void k_stats(const u16* x, float* mean, float* rstd) {
    int r = blockIdx.x; __shared__ float s1[64], s2[64];
    float a = 0, b = 0; for (int k = threadIdx.x; k < D; k += 64) { float v = bf2f(x[r * D + k]); a += v; b += v * v; }
    for (int o = 32; o; o >>= 1) { a += __shfl_xor(a, o); b += __shfl_xor(b, o); }
    if (threadIdx.x == 0) { float m = a / D; float v = b / D - m * m; float mb = rbf(m); float sd = rbf(sqrtf(rbf(rbf(v) + 1e-5f))); mean[r] = mb; rstd[r] = 1.0f / sd; }
}
__global__ void k_gemm_ln(const u16* x, const u16* W, int N, const float* mean, const float* rstd, float* C) {
    int n = blockIdx.x * 64 + threadIdx.x, r = blockIdx.y; if (n >= N) return;
    float acc = 0, cs = 0; for (int k = 0; k < D; ++k) { float w = bf2f(W[k * N + n]); acc += bf2f(x[r * D + k]) * w; cs += w; }
    C[(size_t)r * N + n] = (acc - mean[r] * cs) * rstd[r];
}
__global__ void k_attn(const float* qkv, u16* o) {
    int h = blockIdx.x, q = blockIdx.y * 64 + threadIdx.x;
    const float sc = 0.25f * 1.4426950408889634f;
    float qv[16]; for (int d = 0; d < 16; ++d) qv[d] = rbf(qkv[(size_t)q * 1536 + h * 16 + d] * sc);
    float m = -1e30f;
    for (int j = 0; j < S; ++j) { float s = 0; for (int d = 0; d < 16; ++d) s += qv[d] * rbf(qkv[(size_t)j * 1536 + 512 + h * 16 + d]); m = fmaxf(m, s); }
    float l = 0, acc[16] = {};
    for (int j = 0; j < S; ++j) { float s = 0; for (int d = 0; d < 16; ++d) s += qv[d] * rbf(qkv[(size_t)j * 1536 + 512 + h * 16 + d]);
        float p = exp2f(s - m); p = __uint_as_float(__float_as_uint(p) & 0xFFFF0000u); l += p;
        for (int d = 0; d < 16; ++d) acc[d] += p * rbf(qkv[(size_t)j * 1536 + 1024 + h * 16 + d]); }
    for (int d = 0; d < 16; ++d) o[(size_t)q * 512 + h * 16 + d] = f2bf(acc[d] / l);
}
__global__ void k_h(const float* g, const float* u, u16* h) { int i = blockIdx.x * 256 + threadIdx.x; float a = g[i]; h[i] = f2bf(a / (1.f + __expf(-a)) * u[i]); }
__global__ void k_final(const u16* x, const u16* o, const u16* wo, const u16* h, const u16* w2, u16* out) {
    int n = blockIdx.x * 64 + threadIdx.x, r = blockIdx.y;
    float s = 0; for (int k = 0; k < D; ++k) s += bf2f(o[r * D + k]) * bf2f(wo[k * D + n]);
    float f = 0; for (int k = 0; k < F; ++k) f += bf2f(h[r * F + k]) * bf2f(w2[k * D + n]);
    float g = f / (1.f + __expf(-f));
    out[r * D + n] = f2bf(bf2f(x[r * D + n]) + s * g);
}
extern "C" void kernel_launch(void* const* d_in, const int* in_sizes, int n_in, void* d_out, int out_size, void* d_ws, size_t ws_size, hipStream_t stream) {
    const u16* x = (const u16*)d_in[0]; const u16* wqkv = (const u16*)d_in[1]; const u16* wo = (const u16*)d_in[2];
    const u16* w1 = (const u16*)d_in[4]; const u16* w3 = (const u16*)d_in[5]; const u16* w2 = (const u16*)d_in[6];
    char* ws = (char*)d_ws; float* mean = (float*)ws; float* rstd = mean + S; float* qkv = (float*)(ws + (1 << 20)); float* g = (float*)(ws + (16 << 20)); float* u = (float*)(ws + (32 << 20));
    u16* o = (u16*)(ws + (48 << 20)); u16* h = (u16*)(ws + (56 << 20));
    k_stats<<<S, 64, 0, stream>>>(x, mean, rstd);
    k_gemm_ln<<<dim3(1536 / 64, S), 64, 0, stream>>>(x, wqkv, 1536, mean, rstd, qkv);
    k_gemm_ln<<<dim3((F + 63) / 64, S), 64, 0, stream>>>(x, w1, F, mean, rstd, g);
    k_gemm_ln<<<dim3((F + 63) / 64, S), 64, 0, stream>>>(x, w3, F, mean, rstd, u);
    k_attn<<<dim3(H, S / 64), 64, 0, stream>>>(qkv, o);
    k_h<<<S * F / 256, 256, 0, stream>>>(g, u, h);
    k_final<<<dim3(D / 64, S), 64, 0, stream>>>(x, o, wo, h, w2, (u16*)d_out);
}
```

```cpp
#define EARLY_FFN
#define NO_CODE_SPAN
#include <hip/hip_runtime.h>
typedef unsigned short u16;
typedef unsigned long long u64;
typedef float f32x16 __attribute__((ext_vector_type(16)));
typedef float f32x4 __attribute__((ext_vector_type(4)));
typedef float f32x2 __attribute__((ext_vector_type(2)));
typedef __bf16 bf16x8 __attribute__((ext_vector_type(8)));
typedef __bf16 bf16x2 __attribute__((ext_vector_type(2)));
typedef short s16x8 __attribute__((ext_vector_type(8)));
typedef unsigned u32x4 __attribute__((ext_vector_type(4)));
typedef unsigned u32x2 __attribute__((ext_vector_type(2)));
#define LAS __attribute__((address_space(3)))
#define DI __device__ __forceinline__
#define MFMA(a, b, c) __builtin_amdgcn_mfma_f32_32x32x16_bf16(a, b, c, 0, 0, 0)
typedef __attribute__((address_space(1))) u64 gu64;

constexpr int DM = 512, FF = 1376;
constexpr size_t OFF_QF = 0, OFF_KF = 2u << 20, OFF_VP = 4u << 20, OFF_XF = 6u << 20, OFF_OF = 8u << 20, OFF_HF = 10u << 20, OFF_W2F = 16u << 20, OFF_WOF = (17u << 20) + (512u << 10),
                 OFF_F1 = 18u << 20, OFF_F2 = OFF_F1 + 16384, OFF_FX = OFF_F2 + 65536, OFF_ST = OFF_FX + 131072, OFF_FS = OFF_ST + 32768, OFF_EP = OFF_FS + 16384;
constexpr float QSCALE = 0.25f * 1.4426950408889634f;
constexpr int P1_STAGE = 32768, P1_XB = 8192, L_K = 0, L_V = 65536, L_RING = 131072, L_CNT = 131072 + 30720, L_ST = L_CNT + 512, LDS_BYTES = 163840;
constexpr unsigned SPIN_CAP = 1u << 16;
#define CODE_SPAN 0
struct KP { const char *x, *wqkv, *wo, *w1, *w3, *w2; char* ws; char* out;
};
#define STAMPK(i)
#define STAMP_FLUSH()
#define TSARG
#define TSPASS
#define TSDECL
#define STAMP2(slot, i)

DI unsigned cvtpk(float lo, float hi) { f32x2 v = {lo, hi}; bf16x2 b = __builtin_convertvector(v, bf16x2); return __builtin_bit_cast(unsigned, b); }
DI float rbf(float f) { return __uint_as_float(cvtpk(f, f) << 16); }
DI int lane_now() { int l; asm volatile("v_mbcnt_lo_u32_b32 %0, -1, 0\n\tv_mbcnt_hi_u32_b32 %0, -1, %0" : "=v"(l)); return l; }
DI unsigned rfl(unsigned x) { return __builtin_amdgcn_readfirstlane(x); }
DI void glds16(const void* sbase, unsigned voff, unsigned lds_base) {
    asm volatile("s_mov_b32 m0, %2\n\ts_nop 0\n\tglobal_load_lds_dwordx4 %0, %1" :: "v"(voff), "s"(sbase), "s"(lds_base) : "memory"); }
DI void glds16_sc1(const void* sbase, unsigned voff, unsigned lds_base) {
    asm volatile("s_mov_b32 m0, %2\n\ts_nop 0\n\tglobal_load_lds_dwordx4 %0, %1 sc1" :: "v"(voff), "s"(sbase), "s"(lds_base) : "memory"); }
DI bf16x8 tr_frag(const LAS char* p, int off0, int off1) {
    typedef short v4i16_t __attribute__((ext_vector_type(4)));
    v4i16_t a = __builtin_amdgcn_ds_read_tr16_b64_v4i16((LAS v4i16_t*)(p + off0)), b = __builtin_amdgcn_ds_read_tr16_b64_v4i16((LAS v4i16_t*)(p + off1));
    s16x8 r = {a[0], a[1], a[2], a[3], b[0], b[1], b[2], b[3]}; return __builtin_bit_cast(bf16x8, r); }
DI float dot2(unsigned a, unsigned b, float c) {
    return __builtin_amdgcn_fdot2_f32_bf16(__builtin_bit_cast(bf16x2, a), __builtin_bit_cast(bf16x2, b), c, false); }
#define WAITV(n) asm volatile("s_waitcnt vmcnt(" #n ")" ::: "memory")
#define WAITL() __builtin_amdgcn_s_waitcnt(0xC07F)
#define BAR() __builtin_amdgcn_s_barrier()
template <int CTRL> DI float dpp_add(float v) { return v + __uint_as_float(__builtin_amdgcn_update_dpp(0u, __float_as_uint(v), CTRL, 0xF, 0xF, true)); }
DI float wave_sum(float v) {
    v = dpp_add<0xB1>(v); v = dpp_add<0x4E>(v); v = dpp_add<0x141>(v); v = dpp_add<0x140>(v);
    { auto rr = __builtin_amdgcn_permlane16_swap(__float_as_uint(v), __float_as_uint(v), false, false); v = __uint_as_float(rr[0]) + __uint_as_float(rr[1]); }
    { auto rr = __builtin_amdgcn_permlane32_swap(__float_as_uint(v), __float_as_uint(v), false, false); v = __uint_as_float(rr[0]) + __uint_as_float(rr[1]); }
    return v; }
DI float fmul_s(float a, float b) { float r; asm("v_mul_f32 %0, %1, %2" : "=v"(r) : "v"(a), "v"(b)); return r; }
DI float fmulc_s(float a, float c) { float r; asm("v_mul_f32 %0, %2, %1" : "=v"(r) : "v"(a), "s"(c)); return r; }
DI float fadd1_t(float a) { float r; asm("s_nop 0\n\tv_add_f32 %0, 1.0, %1" : "=v"(r) : "v"(a)); return r; }
DI float fmul_t(float a, float b) { float r; asm("s_nop 0\n\tv_mul_f32 %0, %1, %2" : "=v"(r) : "v"(a), "v"(b)); return r; }
DI float max3f(float a, float b, float c) { return __builtin_fmaxf(__builtin_fmaxf(a, b), c); }
DI float rowmax16(const f32x16& s, float m) { for (int r = 0; r < 16; r += 2) m = max3f(m, s[r], s[r + 1]); return m; }
DI float xhalf_max(float m) { auto rr = __builtin_amdgcn_permlane32_swap(__float_as_uint(m), __float_as_uint(m), false, false); return __builtin_fmaxf(__uint_as_float(rr[0]), __uint_as_float(rr[1])); }
DI __amdgpu_buffer_rsrc_t mkrsrc(const void* base, unsigned bytes) { return __builtin_amdgcn_make_buffer_rsrc((void*)base, 0, bytes, 0x00020000); }
DI u32x4 ld_sc1(__amdgpu_buffer_rsrc_t rs, unsigned off) { return __builtin_bit_cast(u32x4, __builtin_amdgcn_raw_buffer_load_b128(rs, off, 0, 16)); }
DI u32x4 ld_sc1s(__amdgpu_buffer_rsrc_t rs, unsigned voff, unsigned soff) { return __builtin_bit_cast(u32x4, __builtin_amdgcn_raw_buffer_load_b128(rs, voff, soff, 16)); }
DI void st_sc1(__amdgpu_buffer_rsrc_t rs, unsigned off, u32x4 v) { __builtin_amdgcn_raw_buffer_store_b128(v, rs, off, 0, 16); }
DI bf16x8 asb(u32x4 v) { return __builtin_bit_cast(bf16x8, v); }
DI void lds_arrive(char* lds, int off, int lane) { if (lane == 0) __hip_atomic_fetch_add((LAS unsigned*)(lds + off), 1u, __ATOMIC_RELAXED, __HIP_MEMORY_SCOPE_WORKGROUP); }
#define TMO(id)
DI void lds_wait(char* lds, int off, unsigned target) {
    unsigned spins = 0;
    for (; spins < SPIN_CAP; ++spins) {
        if (rfl(__hip_atomic_load((LAS unsigned*)(lds + off), __ATOMIC_RELAXED, __HIP_MEMORY_SCOPE_WORKGROUP)) >= target) break;
        __builtin_amdgcn_s_sleep(1); }
    if (spins == SPIN_CAP) TMO((off - L_CNT) >> 2);
    asm volatile("" ::: "memory"); }
DI void flag_wait(const char* addr, u64 tag, int id = 0) {
    unsigned spins = 0;
    for (; spins < SPIN_CAP; ++spins) {
        const u64 v = __hip_atomic_load((const gu64*)addr, __ATOMIC_RELAXED, __HIP_MEMORY_SCOPE_AGENT);
        if (__ballot(v != tag) == 0ull) break;
        __builtin_amdgcn_s_sleep(4); }
    if (spins == SPIN_CAP) TMO(32 + id);
    asm volatile("" ::: "memory"); }
DI void flag_set(char* addr, u64 tag) { __hip_atomic_store((gu64*)addr, tag, __ATOMIC_RELAXED, __HIP_MEMORY_SCOPE_AGENT); }

#define ATTN_BODY \
  "s_waitcnt lgkmcnt(0)\n\t" \
  "s_setprio 2\n\t" \
  "s_mov_b32 s66, %[young]\n\t" \
  "v_mov_b32 v160, 0xdddd\n\t" \
  "ds_read_b128 v[144:147], %[ka]\n\t" \
  "v_mov_b32 v96, %[negm]\n\t" \
  "v_mov_b32 v97, %[negm]\n\t" \
  "v_mov_b32 v98, %[negm]\n\t" \
  "v_mov_b32 v99, %[negm]\n\t" \
  "v_mov_b32 v100, %[negm]\n\t" \
  "v_mov_b32 v101, %[negm]\n\t" \
  "v_mov_b32 v102, %[negm]\n\t" \
  "v_mov_b32 v103, %[negm]\n\t" \
  "v_mov_b32 v104, %[negm]\n\t" \
  "v_mov_b32 v105, %[negm]\n\t" \
  "v_mov_b32 v106, %[negm]\n\t" \
  "v_mov_b32 v107, %[negm]\n\t" \
  "v_mov_b32 v108, %[negm]\n\t" \
  "v_mov_b32 v109, %[negm]\n\t" \
  "v_mov_b32 v110, %[negm]\n\t" \
  "v_mov_b32 v111, %[negm]\n\t" \
  "v_mov_b32 v148, 0x3f803f80\n\t" \
  "v_mov_b32 v149, 0x3f803f80\n\t" \
  "v_mov_b32 v150, 0x3f803f80\n\t" \
  "v_mov_b32 v151, 0x3f803f80\n\t" \
  "v_mov_b32 v152, 0x3f803f80\n\t" \
  "v_mov_b32 v153, 0x3f803f80\n\t" \
  "v_mov_b32 v154, 0x3f803f80\n\t" \
  "v_mov_b32 v155, 0x3f803f80\n\t" \
  "v_mov_b32 v156, 0x3f803f80\n\t" \
  "v_mov_b32 v157, 0x3f803f80\n\t" \
  "v_mov_b32 v158, 0x3f803f80\n\t" \
  "v_mov_b32 v159, 0x3f803f80\n\t" \
  "v_mov_b32 v136, 0\n\t" \
  "v_mov_b32 v137, 0\n\t" \
  "v_mov_b32 v138, 0\n\t" \
  "v_mov_b32 v139, 0\n\t" \
  "v_mov_b32 v140, 0\n\t" \
  "v_mov_b32 v141, 0\n\t" \
  "v_mov_b32 v142, 0\n\t" \
  "v_mov_b32 v143, 0\n\t" \
  "s_mov_b64 exec, %[msk]\n\t" \
  "ds_read_b128 v[148:151], %[va]\n\t" \
  "ds_read_b128 v[152:155], %[va] offset:512\n\t" \
  "s_mov_b64 exec, -1\n\t" \
  "s_waitcnt lgkmcnt(2)\n\t" \
  "v_mfma_f32_32x32x16_bf16 v[112:127], v[144:147], %[q], v[96:111]\n\t" \
  "ds_read_b128 v[144:147], %[ka] offset:1024\n\t" \
  "s_waitcnt lgkmcnt(0)\n\t" \
  "s_nop 7\n\t" \
  "s_nop 3\n\t" \
  "LOOP%=:\n\t" \
  "s_cmp_eq_u32 %[n], %[nb]\n\t" \
  "s_cbranch_scc0 NOB%=\n\t" \
  "s_mov_b32 s60, 0x40000\n\t" \
  "BSP%=:\n\t" \
  "ds_read_b32 v161, %[seen]\n\t" \
  "s_waitcnt lgkmcnt(0)\n\t" \
  "s_sub_u32 s60, s60, 1\n\t" \
  "s_cbranch_scc1 BOK%=\n\t" \
  "v_cmp_gt_u32 vcc, 2, v161\n\t" \
  "s_cbranch_vccnz BSP%=\n\t" \
  "BOK%=:\n\t" \
  "s_mov_b64 s[62:63], %[kb]\n\t" \
  "s_mov_b64 s[64:65], %[vb]\n\t" \
  "s_mov_b32 s61, %[ldsw]\n\t" \
  "s_add_u32 m0, s61, 24576\n\t" \
  "s_nop 0\n\t" \
  "global_load_lds_dwordx4 %[vo], s[62:63] sc1\n\t" \
  "s_add_u32 m0, s61, 90112\n\t" \
  "s_add_u32 s62, s62, 8192\n\t" \
  "global_load_lds_dwordx4 %[vo], s[64:65] sc1\n\t" \
  "s_addc_u32 s63, s63, 0\n\t" \
  "s_add_u32 s64, s64, 8192\n\t" \
  "s_addc_u32 s65, s65, 0\n\t" \
  "s_add_u32 m0, s61, 32768\n\t" \
  "s_nop 0\n\t" \
  "global_load_lds_dwordx4 %[vo], s[62:63] sc1\n\t" \
  "s_add_u32 m0, s61, 98304\n\t" \
  "s_add_u32 s62, s62, 8192\n\t" \
  "global_load_lds_dwordx4 %[vo], s[64:65] sc1\n\t" \
  "s_addc_u32 s63, s63, 0\n\t" \
  "s_add_u32 s64, s64, 8192\n\t" \
  "s_addc_u32 s65, s65, 0\n\t" \
  "s_add_u32 m0, s61, 40960\n\t" \
  "s_nop 0\n\t" \
  "global_load_lds_dwordx4 %[vo], s[62:63] sc1\n\t" \
  "s_add_u32 m0, s61, 106496\n\t" \
  "s_add_u32 s62, s62, 8192\n\t" \
  "global_load_lds_dwordx4 %[vo], s[64:65] sc1\n\t" \
  "s_addc_u32 s63, s63, 0\n\t" \
  "s_add_u32 s64, s64, 8192\n\t" \
  "s_addc_u32 s65, s65, 0\n\t" \
  "s_add_u32 m0, s61, 49152\n\t" \
  "s_nop 0\n\t" \
  "global_load_lds_dwordx4 %[vo], s[62:63] sc1\n\t" \
  "s_add_u32 m0, s61, 114688\n\t" \
  "s_add_u32 s62, s62, 8192\n\t" \
  "global_load_lds_dwordx4 %[vo], s[64:65] sc1\n\t" \
  "s_addc_u32 s63, s63, 0\n\t" \
  "s_add_u32 s64, s64, 8192\n\t" \
  "s_addc_u32 s65, s65, 0\n\t" \
  "s_add_u32 m0, s61, 57344\n\t" \
  "s_nop 0\n\t" \
  "global_load_lds_dwordx4 %[vo], s[62:63] sc1\n\t" \
  "s_add_u32 m0, s61, 122880\n\t" \
  "s_add_u32 s62, s62, 8192\n\t" \
  "global_load_lds_dwordx4 %[vo], s[64:65] sc1\n\t" \
  "s_addc_u32 s63, s63, 0\n\t" \
  "s_add_u32 s64, s64, 8192\n\t" \
  "s_addc_u32 s65, s65, 0\n\t" \
  "NOB%=:\n\t" \
  "s_cmp_eq_u32 %[n], 6\n\t" \
  "s_cbranch_scc0 NOGATE%=\n\t" \
  "s_waitcnt vmcnt(0)\n\t" \
  "v_mov_b32 v161, 1\n\t" \
  "s_mov_b64 exec, 1\n\t" \
  "ds_add_u32 %[cnt], v161\n\t" \
  "s_mov_b64 exec, -1\n\t" \
  "s_mov_b32 s60, 0x40000\n\t" \
  "GSP%=:\n\t" \
  "ds_read_b32 v161, %[cnt]\n\t" \
  "s_waitcnt lgkmcnt(0)\n\t" \
  "s_sub_u32 s60, s60, 1\n\t" \
  "s_cbranch_scc1 NOGATE%=\n\t" \
  "v_cmp_gt_u32 vcc, 8, v161\n\t" \
  "s_cbranch_vccnz GSP%=\n\t" \
  "s_cmp_eq_u32 %[pub], 0\n\t" \
  "s_cbranch_scc1 NOGATE%=\n\t" \
  "s_mov_b64 exec, 1\n\t" \
  "global_store_dwordx2 %[fso], %[tag], %[ws] sc1\n\t" \
  "s_mov_b64 exec, -1\n\t" \
  "NOGATE%=:\n\t" \
  "s_cmp_eq_u32 s66, 0\n\t" \
  "s_cbranch_scc0 BODYB%=\n\t" \
  "v_exp_f32 v112, v112\n\t" \
  "v_exp_f32 v113, v113\n\t" \
  "v_exp_f32 v114, v114\n\t" \
  "s_waitcnt lgkmcnt(3)\n\t" \
  "s_setprio 3\n\t" \
  "v_smfmac_f32_32x32x32_bf16 %[o], v[156:159], v[136:143], v160\n\t" \
  "s_setprio 1\n\t" \
  "v_exp_f32 v115, v115\n\t" \
  "v_exp_f32 v116, v116\n\t" \
  "v_exp_f32 v117, v117\n\t" \
  "v_exp_f32 v118, v118\n\t" \
  "v_exp_f32 v119, v119\n\t" \
  "s_waitcnt lgkmcnt(2)\n\t" \
  "s_setprio 3\n\t" \
  "v_mfma_f32_32x32x16_bf16 v[128:143], v[144:147], %[q], v[96:111]\n\t" \
  "s_setprio 1\n\t" \
  "ds_read_b128 v[144:147], %[ka] offset:2048\n\t" \
  "v_exp_f32 v120, v120\n\t" \
  "v_exp_f32 v121, v121\n\t" \
  "v_exp_f32 v122, v122\n\t" \
  "v_exp_f32 v123, v123\n\t" \
  "v_exp_f32 v124, v124\n\t" \
  "s_waitcnt lgkmcnt(2)\n\t" \
  "s_setprio 3\n\t" \
  "v_smfmac_f32_32x32x32_bf16 %[o], v[148:151], v[112:119], v160\n\t" \
  "s_setprio 1\n\t" \
  "s_mov_b64 exec, %[msk]\n\t" \
  "ds_read_b128 v[148:151], %[va] offset:1024\n\t" \
  "ds_read_b128 v[156:159], %[va] offset:1536\n\t" \
  "s_mov_b64 exec, -1\n\t" \
  "v_exp_f32 v125, v125\n\t" \
  "v_exp_f32 v126, v126\n\t" \
  "v_exp_f32 v127, v127\n\t" \
  "v_exp_f32 v128, v128\n\t" \
  "v_exp_f32 v129, v129\n\t" \
  "v_exp_f32 v130, v130\n\t" \
  "s_waitcnt lgkmcnt(3)\n\t" \
  "s_setprio 3\n\t" \
  "v_smfmac_f32_32x32x32_bf16 %[o], v[152:155], v[120:127], v160\n\t" \
  "s_setprio 1\n\t" \
  "v_exp_f32 v131, v131\n\t" \
  "v_exp_f32 v132, v132\n\t" \
  "v_exp_f32 v133, v133\n\t" \
  "v_exp_f32 v134, v134\n\t" \
  "v_exp_f32 v135, v135\n\t" \
  "s_waitcnt lgkmcnt(2)\n\t" \
  "s_setprio 3\n\t" \
  "v_mfma_f32_32x32x16_bf16 v[112:127], v[144:147], %[q], v[96:111]\n\t" \
  "s_setprio 1\n\t" \
  "ds_read_b128 v[144:147], %[ka] offset:3072\n\t" \
  "v_exp_f32 v136, v136\n\t" \
  "v_exp_f32 v137, v137\n\t" \
  "v_exp_f32 v138, v138\n\t" \
  "v_exp_f32 v139, v139\n\t" \
  "v_exp_f32 v140, v140\n\t" \
  "s_waitcnt lgkmcnt(2)\n\t" \
  "s_setprio 3\n\t" \
  "v_smfmac_f32_32x32x32_bf16 %[o], v[148:151], v[128:135], v160\n\t" \
  "s_setprio 1\n\t" \
  "s_mov_b64 exec, %[msk]\n\t" \
  "ds_read_b128 v[148:151], %[va] offset:2048\n\t" \
  "ds_read_b128 v[152:155], %[va] offset:2560\n\t" \
  "s_mov_b64 exec, -1\n\t" \
  "v_exp_f32 v141, v141\n\t" \
  "v_exp_f32 v142, v142\n\t" \
  "v_exp_f32 v143, v143\n\t" \
  "v_exp_f32 v112, v112\n\t" \
  "v_exp_f32 v113, v113\n\t" \
  "v_exp_f32 v114, v114\n\t" \
  "s_waitcnt lgkmcnt(3)\n\t" \
  "s_setprio 3\n\t" \
  "v_smfmac_f32_32x32x32_bf16 %[o], v[156:159], v[136:143], v160\n\t" \
  "s_setprio 1\n\t" \
  "v_exp_f32 v115, v115\n\t" \
  "v_exp_f32 v116, v116\n\t" \
  "v_exp_f32 v117, v117\n\t" \
  "v_exp_f32 v118, v118\n\t" \
  "v_exp_f32 v119, v119\n\t" \
  "s_waitcnt lgkmcnt(2)\n\t" \
  "s_setprio 3\n\t" \
  "v_mfma_f32_32x32x16_bf16 v[128:143], v[144:147], %[q], v[96:111]\n\t" \
  "s_setprio 1\n\t" \
  "ds_read_b128 v[144:147], %[ka] offset:4096\n\t" \
  "v_exp_f32 v120, v120\n\t" \
  "v_exp_f32 v121, v121\n\t" \
  "v_exp_f32 v122, v122\n\t" \
  "v_exp_f32 v123, v123\n\t" \
  "v_exp_f32 v124, v124\n\t" \
  "s_waitcnt lgkmcnt(2)\n\t" \
  "s_setprio 3\n\t" \
  "v_smfmac_f32_32x32x32_bf16 %[o], v[148:151], v[112:119], v160\n\t" \
  "s_setprio 1\n\t" \
  "s_mov_b64 exec, %[msk]\n\t" \
  "ds_read_b128 v[148:151], %[va] offset:3072\n\t" \
  "ds_read_b128 v[156:159], %[va] offset:3584\n\t" \
  "s_mov_b64 exec, -1\n\t" \
  "v_exp_f32 v125, v125\n\t" \
  "v_exp_f32 v126, v126\n\t" \
  "v_exp_f32 v127, v127\n\t" \
  "v_exp_f32 v128, v128\n\t" \
  "v_exp_f32 v129, v129\n\t" \
  "v_exp_f32 v130, v130\n\t" \
  "s_waitcnt lgkmcnt(3)\n\t" \
  "s_setprio 3\n\t" \
  "v_smfmac_f32_32x32x32_bf16 %[o], v[152:155], v[120:127], v160\n\t" \
  "s_setprio 1\n\t" \
  "v_exp_f32 v131, v131\n\t" \
  "v_exp_f32 v132, v132\n\t" \
  "v_exp_f32 v133, v133\n\t" \
  "v_exp_f32 v134, v134\n\t" \
  "v_exp_f32 v135, v135\n\t" \
  "s_waitcnt lgkmcnt(2)\n\t" \
  "s_setprio 3\n\t" \
  "v_mfma_f32_32x32x16_bf16 v[112:127], v[144:147], %[q], v[96:111]\n\t" \
  "s_setprio 1\n\t" \
  "ds_read_b128 v[144:147], %[ka] offset:5120\n\t" \
  "v_exp_f32 v136, v136\n\t" \
  "v_exp_f32 v137, v137\n\t" \
  "v_exp_f32 v138, v138\n\t" \
  "v_exp_f32 v139, v139\n\t" \
  "v_exp_f32 v140, v140\n\t" \
  "s_waitcnt lgkmcnt(2)\n\t" \
  "s_setprio 3\n\t" \
  "v_smfmac_f32_32x32x32_bf16 %[o], v[148:151], v[128:135], v160\n\t" \
  "s_setprio 1\n\t" \
  "s_mov_b64 exec, %[msk]\n\t" \
  "ds_read_b128 v[148:151], %[va] offset:4096\n\t" \
  "ds_read_b128 v[152:155], %[va] offset:4608\n\t" \
  "s_mov_b64 exec, -1\n\t" \
  "v_exp_f32 v141, v141\n\t" \
  "v_exp_f32 v142, v142\n\t" \
  "v_exp_f32 v143, v143\n\t" \
  "v_exp_f32 v112, v112\n\t" \
  "v_exp_f32 v113, v113\n\t" \
  "v_exp_f32 v114, v114\n\t" \
  "s_waitcnt lgkmcnt(3)\n\t" \
  "s_setprio 3\n\t" \
  "v_smfmac_f32_32x32x32_bf16 %[o], v[156:159], v[136:143], v160\n\t" \
  "s_setprio 1\n\t" \
  "v_exp_f32 v115, v115\n\t" \
  "v_exp_f32 v116, v116\n\t" \
  "v_exp_f32 v117, v117\n\t" \
  "v_exp_f32 v118, v118\n\t" \
  "v_exp_f32 v119, v119\n\t" \
  "s_waitcnt lgkmcnt(2)\n\t" \
  "s_setprio 3\n\t" \
  "v_mfma_f32_32x32x16_bf16 v[128:143], v[144:147], %[q], v[96:111]\n\t" \
  "s_setprio 1\n\t" \
  "ds_read_b128 v[144:147], %[ka] offset:6144\n\t" \
  "v_exp_f32 v120, v120\n\t" \
  "v_exp_f32 v121, v121\n\t" \
  "v_exp_f32 v122, v122\n\t" \
  "v_exp_f32 v123, v123\n\t" \
  "v_exp_f32 v124, v124\n\t" \
  "s_waitcnt lgkmcnt(2)\n\t" \
  "s_setprio 3\n\t" \
  "v_smfmac_f32_32x32x32_bf16 %[o], v[148:151], v[112:119], v160\n\t" \
  "s_setprio 1\n\t" \
  "s_mov_b64 exec, %[msk]\n\t" \
  "ds_read_b128 v[148:151], %[va] offset:5120\n\t" \
  "ds_read_b128 v[156:159], %[va] offset:5632\n\t" \
  "s_mov_b64 exec, -1\n\t" \
  "v_exp_f32 v125, v125\n\t" \
  "v_exp_f32 v126, v126\n\t" \
  "v_exp_f32 v127, v127\n\t" \
  "v_exp_f32 v128, v128\n\t" \
  "v_exp_f32 v129, v129\n\t" \
  "v_exp_f32 v130, v130\n\t" \
  "s_waitcnt lgkmcnt(3)\n\t" \
  "s_setprio 3\n\t" \
  "v_smfmac_f32_32x32x32_bf16 %[o], v[152:155], v[120:127], v160\n\t" \
  "s_setprio 1\n\t" \
  "v_exp_f32 v131, v131\n\t" \
  "v_exp_f32 v132, v132\n\t" \
  "v_exp_f32 v133, v133\n\t" \
  "v_exp_f32 v134, v134\n\t" \
  "v_exp_f32 v135, v135\n\t" \
  "s_waitcnt lgkmcnt(2)\n\t" \
  "s_setprio 3\n\t" \
  "v_mfma_f32_32x32x16_bf16 v[112:127], v[144:147], %[q], v[96:111]\n\t" \
  "s_setprio 1\n\t" \
  "ds_read_b128 v[144:147], %[ka] offset:7168\n\t" \
  "v_exp_f32 v136, v136\n\t" \
  "v_exp_f32 v137, v137\n\t" \
  "v_exp_f32 v138, v138\n\t" \
  "v_exp_f32 v139, v139\n\t" \
  "v_exp_f32 v140, v140\n\t" \
  "s_waitcnt lgkmcnt(2)\n\t" \
  "s_setprio 3\n\t" \
  "v_smfmac_f32_32x32x32_bf16 %[o], v[148:151], v[128:135], v160\n\t" \
  "s_setprio 1\n\t" \
  "s_mov_b64 exec, %[msk]\n\t" \
  "ds_read_b128 v[148:151], %[va] offset:6144\n\t" \
  "ds_read_b128 v[152:155], %[va] offset:6656\n\t" \
  "s_mov_b64 exec, -1\n\t" \
  "v_exp_f32 v141, v141\n\t" \
  "v_exp_f32 v142, v142\n\t" \
  "v_exp_f32 v143, v143\n\t" \
  "v_exp_f32 v112, v112\n\t" \
  "v_exp_f32 v113, v113\n\t" \
  "v_exp_f32 v114, v114\n\t" \
  "s_waitcnt lgkmcnt(3)\n\t" \
  "s_setprio 3\n\t" \
  "v_smfmac_f32_32x32x32_bf16 %[o], v[156:159], v[136:143], v160\n\t" \
  "s_setprio 1\n\t" \
  "v_exp_f32 v115, v115\n\t" \
  "v_exp_f32 v116, v116\n\t" \
  "v_exp_f32 v117, v117\n\t" \
  "v_exp_f32 v118, v118\n\t" \
  "v_exp_f32 v119, v119\n\t" \
  "s_waitcnt lgkmcnt(2)\n\t" \
  "s_setprio 3\n\t" \
  "v_mfma_f32_32x32x16_bf16 v[128:143], v[144:147], %[q], v[96:111]\n\t" \
  "s_setprio 1\n\t" \
  "ds_read_b128 v[144:147], %[ka] offset:8192\n\t" \
  "v_exp_f32 v120, v120\n\t" \
  "v_exp_f32 v121, v121\n\t" \
  "v_exp_f32 v122, v122\n\t" \
  "v_exp_f32 v123, v123\n\t" \
  "v_exp_f32 v124, v124\n\t" \
  "s_waitcnt lgkmcnt(2)\n\t" \
  "s_setprio 3\n\t" \
  "v_smfmac_f32_32x32x32_bf16 %[o], v[148:151], v[112:119], v160\n\t" \
  "s_setprio 1\n\t" \
  "s_mov_b64 exec, %[msk]\n\t" \
  "ds_read_b128 v[148:151], %[va] offset:7168\n\t" \
  "ds_read_b128 v[156:159], %[va] offset:7680\n\t" \
  "s_mov_b64 exec, -1\n\t" \
  "v_exp_f32 v125, v125\n\t" \
  "v_exp_f32 v126, v126\n\t" \
  "v_exp_f32 v127, v127\n\t" \
  "v_exp_f32 v128, v128\n\t" \
  "v_exp_f32 v129, v129\n\t" \
  "v_exp_f32 v130, v130\n\t" \
  "s_waitcnt lgkmcnt(3)\n\t" \
  "s_setprio 3\n\t" \
  "v_smfmac_f32_32x32x32_bf16 %[o], v[152:155], v[120:127], v160\n\t" \
  "s_setprio 1\n\t" \
  "v_exp_f32 v131, v131\n\t" \
  "v_exp_f32 v132, v132\n\t" \
  "v_exp_f32 v133, v133\n\t" \
  "v_exp_f32 v134, v134\n\t" \
  "v_exp_f32 v135, v135\n\t" \
  "s_waitcnt lgkmcnt(2)\n\t" \
  "s_setprio 3\n\t" \
  "v_mfma_f32_32x32x16_bf16 v[112:127], v[144:147], %[q], v[96:111]\n\t" \
  "s_setprio 1\n\t" \
  "ds_read_b128 v[144:147], %[ka] offset:9216\n\t" \
  "v_exp_f32 v136, v136\n\t" \
  "v_exp_f32 v137, v137\n\t" \
  "v_exp_f32 v138, v138\n\t" \
  "v_exp_f32 v139, v139\n\t" \
  "v_exp_f32 v140, v140\n\t" \
  "s_waitcnt lgkmcnt(2)\n\t" \
  "s_setprio 3\n\t" \
  "v_smfmac_f32_32x32x32_bf16 %[o], v[148:151], v[128:135], v160\n\t" \
  "s_setprio 1\n\t" \
  "s_mov_b64 exec, %[msk]\n\t" \
  "ds_read_b128 v[148:151], %[va] offset:8192\n\t" \
  "ds_read_b128 v[152:155], %[va] offset:8704\n\t" \
  "s_mov_b64 exec, -1\n\t" \
  "v_exp_f32 v141, v141\n\t" \
  "v_exp_f32 v142, v142\n\t" \
  "v_exp_f32 v143, v143\n\t" \
  "s_branch BODYE%=\n\t" \
  "BODYB%=:\n\t" \
  "v_exp_f32 v112, v112\n\t" \
  "v_exp_f32 v113, v113\n\t" \
  "v_exp_f32 v114, v114\n\t" \
  "s_waitcnt lgkmcnt(3)\n\t" \
  "s_setprio 3\n\t" \
  "v_smfmac_f32_32x32x32_bf16 %[o], v[156:159], v[136:143], v160\n\t" \
  "s_setprio 2\n\t" \
  "v_exp_f32 v115, v115\n\t" \
  "v_exp_f32 v116, v116\n\t" \
  "v_exp_f32 v117, v117\n\t" \
  "v_exp_f32 v118, v118\n\t" \
  "v_exp_f32 v119, v119\n\t" \
  "s_waitcnt lgkmcnt(2)\n\t" \
  "s_setprio 3\n\t" \
  "v_mfma_f32_32x32x16_bf16 v[128:143], v[144:147], %[q], v[96:111]\n\t" \
  "s_setprio 2\n\t" \
  "ds_read_b128 v[144:147], %[ka] offset:2048\n\t" \
  "v_exp_f32 v120, v120\n\t" \
  "v_exp_f32 v121, v121\n\t" \
  "v_exp_f32 v122, v122\n\t" \
  "v_exp_f32 v123, v123\n\t" \
  "v_exp_f32 v124, v124\n\t" \
  "s_waitcnt lgkmcnt(2)\n\t" \
  "s_setprio 3\n\t" \
  "v_smfmac_f32_32x32x32_bf16 %[o], v[148:151], v[112:119], v160\n\t" \
  "s_setprio 2\n\t" \
  "s_mov_b64 exec, %[msk]\n\t" \
  "ds_read_b128 v[148:151], %[va] offset:1024\n\t" \
  "ds_read_b128 v[156:159], %[va] offset:1536\n\t" \
  "s_mov_b64 exec, -1\n\t" \
  "v_exp_f32 v125, v125\n\t" \
  "v_exp_f32 v126, v126\n\t" \
  "v_exp_f32 v127, v127\n\t" \
  "v_exp_f32 v128, v128\n\t" \
  "v_exp_f32 v129, v129\n\t" \
  "v_exp_f32 v130, v130\n\t" \
  "s_waitcnt lgkmcnt(3)\n\t" \
  "s_setprio 3\n\t" \
  "v_smfmac_f32_32x32x32_bf16 %[o], v[152:155], v[120:127], v160\n\t" \
  "s_setprio 2\n\t" \
  "v_exp_f32 v131, v131\n\t" \
  "v_exp_f32 v132, v132\n\t" \
  "v_exp_f32 v133, v133\n\t" \
  "v_exp_f32 v134, v134\n\t" \
  "v_exp_f32 v135, v135\n\t" \
  "s_waitcnt lgkmcnt(2)\n\t" \
  "s_setprio 3\n\t" \
  "v_mfma_f32_32x32x16_bf16 v[112:127], v[144:147], %[q], v[96:111]\n\t" \
  "s_setprio 2\n\t" \
  "ds_read_b128 v[144:147], %[ka] offset:3072\n\t" \
  "v_exp_f32 v136, v136\n\t" \
  "v_exp_f32 v137, v137\n\t" \
  "v_exp_f32 v138, v138\n\t" \
  "v_exp_f32 v139, v139\n\t" \
  "v_exp_f32 v140, v140\n\t" \
  "s_waitcnt lgkmcnt(2)\n\t" \
  "s_setprio 3\n\t" \
  "v_smfmac_f32_32x32x32_bf16 %[o], v[148:151], v[128:135], v160\n\t" \
  "s_setprio 2\n\t" \
  "s_mov_b64 exec, %[msk]\n\t" \
  "ds_read_b128 v[148:151], %[va] offset:2048\n\t" \
  "ds_read_b128 v[152:155], %[va] offset:2560\n\t" \
  "s_mov_b64 exec, -1\n\t" \
  "v_exp_f32 v141, v141\n\t" \
  "v_exp_f32 v142, v142\n\t" \
  "v_exp_f32 v143, v143\n\t" \
  "v_exp_f32 v112, v112\n\t" \
  "v_exp_f32 v113, v113\n\t" \
  "v_exp_f32 v114, v114\n\t" \
  "s_waitcnt lgkmcnt(3)\n\t" \
  "s_setprio 3\n\t" \
  "v_smfmac_f32_32x32x32_bf16 %[o], v[156:159], v[136:143], v160\n\t" \
  "s_setprio 2\n\t" \
  "v_exp_f32 v115, v115\n\t" \
  "v_exp_f32 v116, v116\n\t" \
  "v_exp_f32 v117, v117\n\t" \
  "v_exp_f32 v118, v118\n\t" \
  "v_exp_f32 v119, v119\n\t" \
  "s_waitcnt lgkmcnt(2)\n\t" \
  "s_setprio 3\n\t" \
  "v_mfma_f32_32x32x16_bf16 v[128:143], v[144:147], %[q], v[96:111]\n\t" \
  "s_setprio 2\n\t" \
  "ds_read_b128 v[144:147], %[ka] offset:4096\n\t" \
  "v_exp_f32 v120, v120\n\t" \
  "v_exp_f32 v121, v121\n\t" \
  "v_exp_f32 v122, v122\n\t" \
  "v_exp_f32 v123, v123\n\t" \
  "v_exp_f32 v124, v124\n\t" \
  "s_waitcnt lgkmcnt(2)\n\t" \
  "s_setprio 3\n\t" \
  "v_smfmac_f32_32x32x32_bf16 %[o], v[148:151], v[112:119], v160\n\t" \
  "s_setprio 2\n\t" \
  "s_mov_b64 exec, %[msk]\n\t" \
  "ds_read_b128 v[148:151], %[va] offset:3072\n\t" \
  "ds_read_b128 v[156:159], %[va] offset:3584\n\t" \
  "s_mov_b64 exec, -1\n\t" \
  "v_exp_f32 v125, v125\n\t" \
  "v_exp_f32 v126, v126\n\t" \
  "v_exp_f32 v127, v127\n\t" \
  "v_exp_f32 v128, v128\n\t" \
  "v_exp_f32 v129, v129\n\t" \
  "v_exp_f32 v130, v130\n\t" \
  "s_waitcnt lgkmcnt(3)\n\t" \
  "s_setprio 3\n\t" \
  "v_smfmac_f32_32x32x32_bf16 %[o], v[152:155], v[120:127], v160\n\t" \
  "s_setprio 2\n\t" \
  "v_exp_f32 v131, v131\n\t" \
  "v_exp_f32 v132, v132\n\t" \
  "v_exp_f32 v133, v133\n\t" \
  "v_exp_f32 v134, v134\n\t" \
  "v_exp_f32 v135, v135\n\t" \
  "s_waitcnt lgkmcnt(2)\n\t" \
  "s_setprio 3\n\t" \
  "v_mfma_f32_32x32x16_bf16 v[112:127], v[144:147], %[q], v[96:111]\n\t" \
  "s_setprio 2\n\t" \
  "ds_read_b128 v[144:147], %[ka] offset:5120\n\t" \
  "v_exp_f32 v136, v136\n\t" \
  "v_exp_f32 v137, v137\n\t" \
  "v_exp_f32 v138, v138\n\t" \
  "v_exp_f32 v139, v139\n\t" \
  "v_exp_f32 v140, v140\n\t" \
  "s_waitcnt lgkmcnt(2)\n\t" \
  "s_setprio 3\n\t" \
  "v_smfmac_f32_32x32x32_bf16 %[o], v[148:151], v[128:135], v160\n\t" \
  "s_setprio 2\n\t" \
  "s_mov_b64 exec, %[msk]\n\t" \
  "ds_read_b128 v[148:151], %[va] offset:4096\n\t" \
  "ds_read_b128 v[152:155], %[va] offset:4608\n\t" \
  "s_mov_b64 exec, -1\n\t" \
  "v_exp_f32 v141, v141\n\t" \
  "v_exp_f32 v142, v142\n\t" \
  "v_exp_f32 v143, v143\n\t" \
  "v_exp_f32 v112, v112\n\t" \
  "v_exp_f32 v113, v113\n\t" \
  "v_exp_f32 v114, v114\n\t" \
  "s_waitcnt lgkmcnt(3)\n\t" \
  "s_setprio 3\n\t" \
  "v_smfmac_f32_32x32x32_bf16 %[o], v[156:159], v[136:143], v160\n\t" \
  "s_setprio 2\n\t" \
  "v_exp_f32 v115, v115\n\t" \
  "v_exp_f32 v116, v116\n\t" \
  "v_exp_f32 v117, v117\n\t" \
  "v_exp_f32 v118, v118\n\t" \
  "v_exp_f32 v119, v119\n\t" \
  "s_waitcnt lgkmcnt(2)\n\t" \
  "s_setprio 3\n\t" \
  "v_mfma_f32_32x32x16_bf16 v[128:143], v[144:147], %[q], v[96:111]\n\t" \
  "s_setprio 2\n\t" \
  "ds_read_b128 v[144:147], %[ka] offset:6144\n\t" \
  "v_exp_f32 v120, v120\n\t" \
  "v_exp_f32 v121, v121\n\t" \
  "v_exp_f32 v122, v122\n\t" \
  "v_exp_f32 v123, v123\n\t" \
  "v_exp_f32 v124, v124\n\t" \
  "s_waitcnt lgkmcnt(2)\n\t" \
  "s_setprio 3\n\t" \
  "v_smfmac_f32_32x32x32_bf16 %[o], v[148:151], v[112:119], v160\n\t" \
  "s_setprio 2\n\t" \
  "s_mov_b64 exec, %[msk]\n\t" \
  "ds_read_b128 v[148:151], %[va] offset:5120\n\t" \
  "ds_read_b128 v[156:159], %[va] offset:5632\n\t" \
  "s_mov_b64 exec, -1\n\t" \
  "v_exp_f32 v125, v125\n\t" \
  "v_exp_f32 v126, v126\n\t" \
  "v_exp_f32 v127, v127\n\t" \
  "v_exp_f32 v128, v128\n\t" \
  "v_exp_f32 v129, v129\n\t" \
  "v_exp_f32 v130, v130\n\t" \
  "s_waitcnt lgkmcnt(3)\n\t" \
  "s_setprio 3\n\t" \
  "v_smfmac_f32_32x32x32_bf16 %[o], v[152:155], v[120:127], v160\n\t" \
  "s_setprio 2\n\t" \
  "v_exp_f32 v131, v131\n\t" \
  "v_exp_f32 v132, v132\n\t" \
  "v_exp_f32 v133, v133\n\t" \
  "v_exp_f32 v134, v134\n\t" \
  "v_exp_f32 v135, v135\n\t" \
  "s_waitcnt lgkmcnt(2)\n\t" \
  "s_setprio 3\n\t" \
  "v_mfma_f32_32x32x16_bf16 v[112:127], v[144:147], %[q], v[96:111]\n\t" \
  "s_setprio 2\n\t" \
  "ds_read_b128 v[144:147], %[ka] offset:7168\n\t" \
  "v_exp_f32 v136, v136\n\t" \
  "v_exp_f32 v137, v137\n\t" \
  "v_exp_f32 v138, v138\n\t" \
  "v_exp_f32 v139, v139\n\t" \
  "v_exp_f32 v140, v140\n\t" \
  "s_waitcnt lgkmcnt(2)\n\t" \
  "s_setprio 3\n\t" \
  "v_smfmac_f32_32x32x32_bf16 %[o], v[148:151], v[128:135], v160\n\t" \
  "s_setprio 2\n\t" \
  "s_mov_b64 exec, %[msk]\n\t" \
  "ds_read_b128 v[148:151], %[va] offset:6144\n\t" \
  "ds_read_b128 v[152:155], %[va] offset:6656\n\t" \
  "s_mov_b64 exec, -1\n\t" \
  "v_exp_f32 v141, v141\n\t" \
  "v_exp_f32 v142, v142\n\t" \
  "v_exp_f32 v143, v143\n\t" \
  "v_exp_f32 v112, v112\n\t" \
  "v_exp_f32 v113, v113\n\t" \
  "v_exp_f32 v114, v114\n\t" \
  "s_waitcnt lgkmcnt(3)\n\t" \
  "s_setprio 3\n\t" \
  "v_smfmac_f32_32x32x32_bf16 %[o], v[156:159], v[136:143], v160\n\t" \
  "s_setprio 2\n\t" \
  "v_exp_f32 v115, v115\n\t" \
  "v_exp_f32 v116, v116\n\t" \
  "v_exp_f32 v117, v117\n\t" \
  "v_exp_f32 v118, v118\n\t" \
  "v_exp_f32 v119, v119\n\t" \
  "s_waitcnt lgkmcnt(2)\n\t" \
  "s_setprio 3\n\t" \
  "v_mfma_f32_32x32x16_bf16 v[128:143], v[144:147], %[q], v[96:111]\n\t" \
  "s_setprio 2\n\t" \
  "ds_read_b128 v[144:147], %[ka] offset:8192\n\t" \
  "v_exp_f32 v120, v120\n\t" \
  "v_exp_f32 v121, v121\n\t" \
  "v_exp_f32 v122, v122\n\t" \
  "v_exp_f32 v123, v123\n\t" \
  "v_exp_f32 v124, v124\n\t" \
  "s_waitcnt lgkmcnt(2)\n\t" \
  "s_setprio 3\n\t" \
  "v_smfmac_f32_32x32x32_bf16 %[o], v[148:151], v[112:119], v160\n\t" \
  "s_setprio 2\n\t" \
  "s_mov_b64 exec, %[msk]\n\t" \
  "ds_read_b128 v[148:151], %[va] offset:7168\n\t" \
  "ds_read_b128 v[156:159], %[va] offset:7680\n\t" \
  "s_mov_b64 exec, -1\n\t" \
  "v_exp_f32 v125, v125\n\t" \
  "v_exp_f32 v126, v126\n\t" \
  "v_exp_f32 v127, v127\n\t" \
  "v_exp_f32 v128, v128\n\t" \
  "v_exp_f32 v129, v129\n\t" \
  "v_exp_f32 v130, v130\n\t" \
  "s_waitcnt lgkmcnt(3)\n\t" \
  "s_setprio 3\n\t" \
  "v_smfmac_f32_32x32x32_bf16 %[o], v[152:155], v[120:127], v160\n\t" \
  "s_setprio 2\n\t" \
  "v_exp_f32 v131, v131\n\t" \
  "v_exp_f32 v132, v132\n\t" \
  "v_exp_f32 v133, v133\n\t" \
  "v_exp_f32 v134, v134\n\t" \
  "v_exp_f32 v135, v135\n\t" \
  "s_waitcnt lgkmcnt(2)\n\t" \
  "s_setprio 3\n\t" \
  "v_mfma_f32_32x32x16_bf16 v[112:127], v[144:147], %[q], v[96:111]\n\t" \
  "s_setprio 2\n\t" \
  "ds_read_b128 v[144:147], %[ka] offset:9216\n\t" \
  "v_exp_f32 v136, v136\n\t" \
  "v_exp_f32 v137, v137\n\t" \
  "v_exp_f32 v138, v138\n\t" \
  "v_exp_f32 v139, v139\n\t" \
  "v_exp_f32 v140, v140\n\t" \
  "s_waitcnt lgkmcnt(2)\n\t" \
  "s_setprio 3\n\t" \
  "v_smfmac_f32_32x32x32_bf16 %[o], v[148:151], v[128:135], v160\n\t" \
  "s_setprio 2\n\t" \
  "s_mov_b64 exec, %[msk]\n\t" \
  "ds_read_b128 v[148:151], %[va] offset:8192\n\t" \
  "ds_read_b128 v[152:155], %[va] offset:8704\n\t" \
  "s_mov_b64 exec, -1\n\t" \
  "v_exp_f32 v141, v141\n\t" \
  "v_exp_f32 v142, v142\n\t" \
  "v_exp_f32 v143, v143\n\t" \
  "BODYE%=:\n\t" \
  "v_add_u32 %[ka], 8192, %[ka]\n\t" \
  "v_add_u32 %[va], 8192, %[va]\n\t" \
  "s_sub_u32 %[n], %[n], 1\n\t" \
  "s_cmp_eq_u32 %[n], 3\n\t" \
  "s_cbranch_scc0 NSW%=\n\t" \
  "s_xor_b32 s66, s66, 1\n\t" \
  "NSW%=:\n\t" \
  "s_cmp_lg_u32 %[n], 0\n\t" \
  "s_cbranch_scc1 LOOP%=\n\t" \
  "s_waitcnt lgkmcnt(0)\n\t" \
  "s_nop 1\n\t" \
  "v_smfmac_f32_32x32x32_bf16 %[o], v[156:159], v[136:143], v160\n\t" \
  "s_nop 7\n\t" \
  "s_nop 7\n\t" \
  ""
#define ATTN_CLOB "v96","v97","v98","v99","v100","v101","v102","v103","v104","v105","v106","v107","v108","v109","v110","v111","v112","v113","v114","v115","v116","v117","v118","v119","v120","v121","v122","v123","v124","v125","v126","v127","v128","v129","v130","v131","v132","v133","v134","v135","v136","v137","v138","v139","v140","v141","v142","v143","v144","v145","v146","v147","v148","v149","v150","v151","v152","v153","v154","v155","v156","v157","v158","v159","v160","v161","v162","v163","s60","s61","s62","s63","s64","s65","s66","vcc"
#define FFN1_BODY_3_2 \
  "s_waitcnt lgkmcnt(0)\n\t" \
  "s_setprio 3\n\t" \
  "s_mov_b64 s[60:61], %[b0]\n\t" \
  "s_mov_b64 s[62:63], %[b1]\n\t" \
  "s_mov_b64 s[64:65], %[xb]\n\t" \
  "s_mov_b32 s66, 0\n\t" \
  "s_mov_b32 s68, 0\n\t" \
  "s_mov_b32 s69, 1\n\t" \
  "s_mov_b32 s70, 8\n\t" \
  "v_mov_b32 v167, 1\n\t" \
  "s_add_u32 m0, s66, %[m0a]\n\t" \
  "s_nop 0\n\t" \
  "global_load_lds_dwordx4 %[d0], s[60:61]\n\t" \
  "s_add_u32 m0, s66, %[m0b]\n\t" \
  "s_nop 0\n\t" \
  "global_load_lds_dwordx4 %[d1], s[62:63]\n\t" \
  "s_cmp_lt_u32 s68, 31\n\t" \
  "s_cselect_b32 s71, 44032, 0\n\t" \
  "s_addc_u32 s68, s68, 0\n\t" \
  "s_add_u32 s60, s60, s71\n\t" \
  "s_addc_u32 s61, s61, 0\n\t" \
  "s_add_u32 s62, s62, s71\n\t" \
  "s_addc_u32 s63, s63, 0\n\t" \
  "s_add_u32 s66, s66, 6144\n\t" \
  "s_cmp_eq_u32 s66, 30720\n\t" \
  "s_cselect_b32 s66, 0, s66\n\t" \
  "global_load_dwordx4 v[148:151], %[xo], s[64:65] offset:0 sc1\n\t" \
  "s_add_u32 m0, s66, %[m0a]\n\t" \
  "s_nop 0\n\t" \
  "global_load_lds_dwordx4 %[d0], s[60:61]\n\t" \
  "s_add_u32 m0, s66, %[m0b]\n\t" \
  "s_nop 0\n\t" \
  "global_load_lds_dwordx4 %[d1], s[62:63]\n\t" \
  "s_cmp_lt_u32 s68, 31\n\t" \
  "s_cselect_b32 s71, 44032, 0\n\t" \
  "s_addc_u32 s68, s68, 0\n\t" \
  "s_add_u32 s60, s60, s71\n\t" \
  "s_addc_u32 s61, s61, 0\n\t" \
  "s_add_u32 s62, s62, s71\n\t" \
  "s_addc_u32 s63, s63, 0\n\t" \
  "s_add_u32 s66, s66, 6144\n\t" \
  "s_cmp_eq_u32 s66, 30720\n\t" \
  "s_cselect_b32 s66, 0, s66\n\t" \
  "global_load_dwordx4 v[152:155], %[xo], s[64:65] offset:1024 sc1\n\t" \
  "s_add_u32 m0, s66, %[m0a]\n\t" \
  "s_nop 0\n\t" \
  "global_load_lds_dwordx4 %[d0], s[60:61]\n\t" \
  "s_add_u32 m0, s66, %[m0b]\n\t" \
  "s_nop 0\n\t" \
  "global_load_lds_dwordx4 %[d1], s[62:63]\n\t" \
  "s_cmp_lt_u32 s68, 31\n\t" \
  "s_cselect_b32 s71, 44032, 0\n\t" \
  "s_addc_u32 s68, s68, 0\n\t" \
  "s_add_u32 s60, s60, s71\n\t" \
  "s_addc_u32 s61, s61, 0\n\t" \
  "s_add_u32 s62, s62, s71\n\t" \
  "s_addc_u32 s63, s63, 0\n\t" \
  "s_add_u32 s66, s66, 6144\n\t" \
  "s_cmp_eq_u32 s66, 30720\n\t" \
  "s_cselect_b32 s66, 0, s66\n\t" \
  "global_load_dwordx4 v[156:159], %[xo], s[64:65] offset:2048 sc1\n\t" \
  "s_add_u32 m0, s66, %[m0a]\n\t" \
  "s_nop 0\n\t" \
  "global_load_lds_dwordx4 %[d0], s[60:61]\n\t" \
  "s_add_u32 m0, s66, %[m0b]\n\t" \
  "s_nop 0\n\t" \
  "global_load_lds_dwordx4 %[d1], s[62:63]\n\t" \
  "s_cmp_lt_u32 s68, 31\n\t" \
  "s_cselect_b32 s71, 44032, 0\n\t" \
  "s_addc_u32 s68, s68, 0\n\t" \
  "s_add_u32 s60, s60, s71\n\t" \
  "s_addc_u32 s61, s61, 0\n\t" \
  "s_add_u32 s62, s62, s71\n\t" \
  "s_addc_u32 s63, s63, 0\n\t" \
  "s_add_u32 s66, s66, 6144\n\t" \
  "s_cmp_eq_u32 s66, 30720\n\t" \
  "s_cselect_b32 s66, 0, s66\n\t" \
  "global_load_dwordx4 v[160:163], %[xo], s[64:65] offset:3072 sc1\n\t" \
  "s_add_u32 s64, s64, 4096\n\t" \
  "s_addc_u32 s65, s65, 0\n\t" \
  "s_waitcnt vmcnt(7)\n\t" \
  "s_mov_b64 exec, 1\n\t" \
  "ds_add_u32 %[cntw], v167\n\t" \
  "s_mov_b64 exec, -1\n\t" \
  "ds_read_b32 v166, %[cntr]\n\t" \
  "s_waitcnt lgkmcnt(0)\n\t" \
  "v_cmp_gt_u32 vcc, s69, v166\n\t" \
  "s_cbranch_vccz OKA%=\n\t" \
  "s_mov_b32 s72, 0x40000\n\t" \
  "SPA%=:\n\t" \
  "ds_read_b32 v166, %[cntr]\n\t" \
  "s_waitcnt lgkmcnt(0)\n\t" \
  "s_sub_u32 s72, s72, 1\n\t" \
  "s_cbranch_scc1 OKA%=\n\t" \
  "v_cmp_gt_u32 vcc, s69, v166\n\t" \
  "s_cbranch_vccnz SPA%=\n\t" \
  "OKA%=:\n\t" \
  "v_mov_b32 v164, %[wa0]\n\t" \
  "v_mov_b32 v165, %[wa1]\n\t" \
  "ds_read_b64_tr_b16 v[120:121], v164 offset:0\n\t" \
  "ds_read_b64_tr_b16 v[122:123], v164 offset:256\n\t" \
  "ds_read_b64_tr_b16 v[124:125], v165 offset:0\n\t" \
  "ds_read_b64_tr_b16 v[126:127], v165 offset:256\n\t" \
  "ds_read_b64_tr_b16 v[128:129], v164 offset:1024\n\t" \
  "ds_read_b64_tr_b16 v[130:131], v164 offset:1280\n\t" \
  "ds_read_b64_tr_b16 v[132:133], v165 offset:1024\n\t" \
  "ds_read_b64_tr_b16 v[134:135], v165 offset:1280\n\t" \
  "ds_read_b64_tr_b16 v[136:137], v164 offset:2048\n\t" \
  "ds_read_b64_tr_b16 v[138:139], v164 offset:2304\n\t" \
  "ds_read_b64_tr_b16 v[140:141], v165 offset:2048\n\t" \
  "ds_read_b64_tr_b16 v[142:143], v165 offset:2304\n\t" \
  "ds_read_b32 v166, %[cntr]\n\t" \
  "s_mov_b32 s67, 6144\n\t" \
  "LOOP%=:\n\t" \
  "s_waitcnt vmcnt(4) lgkmcnt(0)\n\t" \
  "v_mfma_f32_32x32x16_bf16 %[a0], v[120:123], v[148:151], %[a0]\n\t" \
  "s_mov_b64 exec, 1\n\t" \
  "ds_add_u32 %[cntw], v167\n\t" \
  "s_mov_b64 exec, -1\n\t" \
  "v_cmp_gt_u32 vcc, s69, v166\n\t" \
  "s_cbranch_vccz OKL0%=\n\t" \
  "s_mov_b32 s72, 0x40000\n\t" \
  "SPL0%=:\n\t" \
  "ds_read_b32 v166, %[cntr]\n\t" \
  "s_waitcnt lgkmcnt(0)\n\t" \
  "s_sub_u32 s72, s72, 1\n\t" \
  "s_cbranch_scc1 OKL0%=\n\t" \
  "v_cmp_gt_u32 vcc, s69, v166\n\t" \
  "s_cbranch_vccnz SPL0%=\n\t" \
  "OKL0%=:\n\t" \
  "s_add_u32 m0, s66, %[m0a]\n\t" \
  "s_nop 0\n\t" \
  "global_load_lds_dwordx4 %[d0], s[60:61]\n\t" \
  "v_add_u32 v164, s67, %[wa0]\n\t" \
  "v_add_u32 v165, s67, %[wa1]\n\t" \
  "s_add_u32 m0, s66, %[m0b]\n\t" \
  "s_nop 0\n\t" \
  "global_load_lds_dwordx4 %[d1], s[62:63]\n\t" \
  "ds_read_b64_tr_b16 v[120:121], v164 offset:0\n\t" \
  "ds_read_b64_tr_b16 v[122:123], v164 offset:256\n\t" \
  "v_mfma_f32_32x32x16_bf16 %[a1], v[124:127], v[148:151], %[a1]\n\t" \
  "ds_read_b64_tr_b16 v[124:125], v165 offset:0\n\t" \
  "ds_read_b64_tr_b16 v[126:127], v165 offset:256\n\t" \
  "s_nop 5\n\t" \
  "v_mfma_f32_32x32x16_bf16 %[a2], v[128:131], v[148:151], %[a2]\n\t" \
  "ds_read_b64_tr_b16 v[128:129], v164 offset:1024\n\t" \
  "ds_read_b64_tr_b16 v[130:131], v164 offset:1280\n\t" \
  "s_cmp_lt_u32 s68, 31\n\t" \
  "s_cselect_b32 s71, 44032, 0\n\t" \
  "s_addc_u32 s68, s68, 0\n\t" \
  "s_add_u32 s60, s60, s71\n\t" \
  "s_addc_u32 s61, s61, 0\n\t" \
  "s_add_u32 s62, s62, s71\n\t" \
  "s_addc_u32 s63, s63, 0\n\t" \
  "v_mfma_f32_32x32x16_bf16 %[a3], v[132:135], v[148:151], %[a3]\n\t" \
  "ds_read_b64_tr_b16 v[132:133], v165 offset:1024\n\t" \
  "ds_read_b64_tr_b16 v[134:135], v165 offset:1280\n\t" \
  "s_add_u32 s66, s66, 6144\n\t" \
  "s_cmp_eq_u32 s66, 30720\n\t" \
  "s_cselect_b32 s66, 0, s66\n\t" \
  "s_add_u32 s69, s69, 1\n\t" \
  "s_add_u32 s67, s67, 6144\n\t" \
  "s_cmp_eq_u32 s67, 30720\n\t" \
  "s_cselect_b32 s67, 0, s67\n\t" \
  "s_nop 5\n\t" \
  "v_mfma_f32_32x32x16_bf16 %[a4], v[136:139], v[148:151], %[a4]\n\t" \
  "ds_read_b64_tr_b16 v[136:137], v164 offset:2048\n\t" \
  "ds_read_b64_tr_b16 v[138:139], v164 offset:2304\n\t" \
  "ds_read_b64_tr_b16 v[144:145], v165 offset:2048\n\t" \
  "ds_read_b64_tr_b16 v[146:147], v165 offset:2304\n\t" \
  "ds_read_b32 v166, %[cntr]\n\t" \
  "v_mfma_f32_32x32x16_bf16 %[a5], v[140:143], v[148:151], %[a5]\n\t" \
  "global_load_dwordx4 v[148:151], %[xo], s[64:65] offset:0 sc1\n\t" \
  "s_nop 5\n\t" \
  "s_waitcnt vmcnt(4) lgkmcnt(0)\n\t" \
  "v_mfma_f32_32x32x16_bf16 %[a0], v[120:123], v[152:155], %[a0]\n\t" \
  "s_mov_b64 exec, 1\n\t" \
  "ds_add_u32 %[cntw], v167\n\t" \
  "s_mov_b64 exec, -1\n\t" \
  "v_cmp_gt_u32 vcc, s69, v166\n\t" \
  "s_cbranch_vccz OKL1%=\n\t" \
  "s_mov_b32 s72, 0x40000\n\t" \
  "SPL1%=:\n\t" \
  "ds_read_b32 v166, %[cntr]\n\t" \
  "s_waitcnt lgkmcnt(0)\n\t" \
  "s_sub_u32 s72, s72, 1\n\t" \
  "s_cbranch_scc1 OKL1%=\n\t" \
  "v_cmp_gt_u32 vcc, s69, v166\n\t" \
  "s_cbranch_vccnz SPL1%=\n\t" \
  "OKL1%=:\n\t" \
  "s_add_u32 m0, s66, %[m0a]\n\t" \
  "s_nop 0\n\t" \
  "global_load_lds_dwordx4 %[d0], s[60:61]\n\t" \
  "v_add_u32 v164, s67, %[wa0]\n\t" \
  "v_add_u32 v165, s67, %[wa1]\n\t" \
  "s_add_u32 m0, s66, %[m0b]\n\t" \
  "s_nop 0\n\t" \
  "global_load_lds_dwordx4 %[d1], s[62:63]\n\t" \
  "ds_read_b64_tr_b16 v[120:121], v164 offset:0\n\t" \
  "ds_read_b64_tr_b16 v[122:123], v164 offset:256\n\t" \
  "v_mfma_f32_32x32x16_bf16 %[a1], v[124:127], v[152:155], %[a1]\n\t" \
  "ds_read_b64_tr_b16 v[124:125], v165 offset:0\n\t" \
  "ds_read_b64_tr_b16 v[126:127], v165 offset:256\n\t" \
  "s_nop 5\n\t" \
  "v_mfma_f32_32x32x16_bf16 %[a2], v[128:131], v[152:155], %[a2]\n\t" \
  "ds_read_b64_tr_b16 v[128:129], v164 offset:1024\n\t" \
  "ds_read_b64_tr_b16 v[130:131], v164 offset:1280\n\t" \
  "s_cmp_lt_u32 s68, 31\n\t" \
  "s_cselect_b32 s71, 44032, 0\n\t" \
  "s_addc_u32 s68, s68, 0\n\t" \
  "s_add_u32 s60, s60, s71\n\t" \
  "s_addc_u32 s61, s61, 0\n\t" \
  "s_add_u32 s62, s62, s71\n\t" \
  "s_addc_u32 s63, s63, 0\n\t" \
  "v_mfma_f32_32x32x16_bf16 %[a3], v[132:135], v[152:155], %[a3]\n\t" \
  "ds_read_b64_tr_b16 v[132:133], v165 offset:1024\n\t" \
  "ds_read_b64_tr_b16 v[134:135], v165 offset:1280\n\t" \
  "s_add_u32 s66, s66, 6144\n\t" \
  "s_cmp_eq_u32 s66, 30720\n\t" \
  "s_cselect_b32 s66, 0, s66\n\t" \
  "s_add_u32 s69, s69, 1\n\t" \
  "s_add_u32 s67, s67, 6144\n\t" \
  "s_cmp_eq_u32 s67, 30720\n\t" \
  "s_cselect_b32 s67, 0, s67\n\t" \
  "s_nop 5\n\t" \
  "v_mfma_f32_32x32x16_bf16 %[a4], v[136:139], v[152:155], %[a4]\n\t" \
  "ds_read_b64_tr_b16 v[136:137], v164 offset:2048\n\t" \
  "ds_read_b64_tr_b16 v[138:139], v164 offset:2304\n\t" \
  "ds_read_b64_tr_b16 v[140:141], v165 offset:2048\n\t" \
  "ds_read_b64_tr_b16 v[142:143], v165 offset:2304\n\t" \
  "ds_read_b32 v166, %[cntr]\n\t" \
  "v_mfma_f32_32x32x16_bf16 %[a5], v[144:147], v[152:155], %[a5]\n\t" \
  "global_load_dwordx4 v[152:155], %[xo], s[64:65] offset:1024 sc1\n\t" \
  "s_nop 5\n\t" \
  "s_waitcnt vmcnt(4) lgkmcnt(0)\n\t" \
  "v_mfma_f32_32x32x16_bf16 %[a0], v[120:123], v[156:159], %[a0]\n\t" \
  "s_mov_b64 exec, 1\n\t" \
  "ds_add_u32 %[cntw], v167\n\t" \
  "s_mov_b64 exec, -1\n\t" \
  "v_cmp_gt_u32 vcc, s69, v166\n\t" \
  "s_cbranch_vccz OKL2%=\n\t" \
  "s_mov_b32 s72, 0x40000\n\t" \
  "SPL2%=:\n\t" \
  "ds_read_b32 v166, %[cntr]\n\t" \
  "s_waitcnt lgkmcnt(0)\n\t" \
  "s_sub_u32 s72, s72, 1\n\t" \
  "s_cbranch_scc1 OKL2%=\n\t" \
  "v_cmp_gt_u32 vcc, s69, v166\n\t" \
  "s_cbranch_vccnz SPL2%=\n\t" \
  "OKL2%=:\n\t" \
  "s_add_u32 m0, s66, %[m0a]\n\t" \
  "s_nop 0\n\t" \
  "global_load_lds_dwordx4 %[d0], s[60:61]\n\t" \
  "v_add_u32 v164, s67, %[wa0]\n\t" \
  "v_add_u32 v165, s67, %[wa1]\n\t" \
  "s_add_u32 m0, s66, %[m0b]\n\t" \
  "s_nop 0\n\t" \
  "global_load_lds_dwordx4 %[d1], s[62:63]\n\t" \
  "ds_read_b64_tr_b16 v[120:121], v164 offset:0\n\t" \
  "ds_read_b64_tr_b16 v[122:123], v164 offset:256\n\t" \
  "v_mfma_f32_32x32x16_bf16 %[a1], v[124:127], v[156:159], %[a1]\n\t" \
  "ds_read_b64_tr_b16 v[124:125], v165 offset:0\n\t" \
  "ds_read_b64_tr_b16 v[126:127], v165 offset:256\n\t" \
  "s_nop 5\n\t" \
  "v_mfma_f32_32x32x16_bf16 %[a2], v[128:131], v[156:159], %[a2]\n\t" \
  "ds_read_b64_tr_b16 v[128:129], v164 offset:1024\n\t" \
  "ds_read_b64_tr_b16 v[130:131], v164 offset:1280\n\t" \
  "s_cmp_lt_u32 s68, 31\n\t" \
  "s_cselect_b32 s71, 44032, 0\n\t" \
  "s_addc_u32 s68, s68, 0\n\t" \
  "s_add_u32 s60, s60, s71\n\t" \
  "s_addc_u32 s61, s61, 0\n\t" \
  "s_add_u32 s62, s62, s71\n\t" \
  "s_addc_u32 s63, s63, 0\n\t" \
  "v_mfma_f32_32x32x16_bf16 %[a3], v[132:135], v[156:159], %[a3]\n\t" \
  "ds_read_b64_tr_b16 v[132:133], v165 offset:1024\n\t" \
  "ds_read_b64_tr_b16 v[134:135], v165 offset:1280\n\t" \
  "s_add_u32 s66, s66, 6144\n\t" \
  "s_cmp_eq_u32 s66, 30720\n\t" \
  "s_cselect_b32 s66, 0, s66\n\t" \
  "s_add_u32 s69, s69, 1\n\t" \
  "s_add_u32 s67, s67, 6144\n\t" \
  "s_cmp_eq_u32 s67, 30720\n\t" \
  "s_cselect_b32 s67, 0, s67\n\t" \
  "s_nop 5\n\t" \
  "v_mfma_f32_32x32x16_bf16 %[a4], v[136:139], v[156:159], %[a4]\n\t" \
  "ds_read_b64_tr_b16 v[136:137], v164 offset:2048\n\t" \
  "ds_read_b64_tr_b16 v[138:139], v164 offset:2304\n\t" \
  "ds_read_b64_tr_b16 v[144:145], v165 offset:2048\n\t" \
  "ds_read_b64_tr_b16 v[146:147], v165 offset:2304\n\t" \
  "ds_read_b32 v166, %[cntr]\n\t" \
  "v_mfma_f32_32x32x16_bf16 %[a5], v[140:143], v[156:159], %[a5]\n\t" \
  "global_load_dwordx4 v[156:159], %[xo], s[64:65] offset:2048 sc1\n\t" \
  "s_nop 5\n\t" \
  "s_waitcnt vmcnt(4) lgkmcnt(0)\n\t" \
  "v_mfma_f32_32x32x16_bf16 %[a0], v[120:123], v[160:163], %[a0]\n\t" \
  "s_mov_b64 exec, 1\n\t" \
  "ds_add_u32 %[cntw], v167\n\t" \
  "s_mov_b64 exec, -1\n\t" \
  "v_cmp_gt_u32 vcc, s69, v166\n\t" \
  "s_cbranch_vccz OKL3%=\n\t" \
  "s_mov_b32 s72, 0x40000\n\t" \
  "SPL3%=:\n\t" \
  "ds_read_b32 v166, %[cntr]\n\t" \
  "s_waitcnt lgkmcnt(0)\n\t" \
  "s_sub_u32 s72, s72, 1\n\t" \
  "s_cbranch_scc1 OKL3%=\n\t" \
  "v_cmp_gt_u32 vcc, s69, v166\n\t" \
  "s_cbranch_vccnz SPL3%=\n\t" \
  "OKL3%=:\n\t" \
  "s_add_u32 m0, s66, %[m0a]\n\t" \
  "s_nop 0\n\t" \
  "global_load_lds_dwordx4 %[d0], s[60:61]\n\t" \
  "v_add_u32 v164, s67, %[wa0]\n\t" \
  "v_add_u32 v165, s67, %[wa1]\n\t" \
  "s_add_u32 m0, s66, %[m0b]\n\t" \
  "s_nop 0\n\t" \
  "global_load_lds_dwordx4 %[d1], s[62:63]\n\t" \
  "ds_read_b64_tr_b16 v[120:121], v164 offset:0\n\t" \
  "ds_read_b64_tr_b16 v[122:123], v164 offset:256\n\t" \
  "v_mfma_f32_32x32x16_bf16 %[a1], v[124:127], v[160:163], %[a1]\n\t" \
  "ds_read_b64_tr_b16 v[124:125], v165 offset:0\n\t" \
  "ds_read_b64_tr_b16 v[126:127], v165 offset:256\n\t" \
  "s_nop 5\n\t" \
  "v_mfma_f32_32x32x16_bf16 %[a2], v[128:131], v[160:163], %[a2]\n\t" \
  "ds_read_b64_tr_b16 v[128:129], v164 offset:1024\n\t" \
  "ds_read_b64_tr_b16 v[130:131], v164 offset:1280\n\t" \
  "s_cmp_lt_u32 s68, 31\n\t" \
  "s_cselect_b32 s71, 44032, 0\n\t" \
  "s_addc_u32 s68, s68, 0\n\t" \
  "s_add_u32 s60, s60, s71\n\t" \
  "s_addc_u32 s61, s61, 0\n\t" \
  "s_add_u32 s62, s62, s71\n\t" \
  "s_addc_u32 s63, s63, 0\n\t" \
  "v_mfma_f32_32x32x16_bf16 %[a3], v[132:135], v[160:163], %[a3]\n\t" \
  "ds_read_b64_tr_b16 v[132:133], v165 offset:1024\n\t" \
  "ds_read_b64_tr_b16 v[134:135], v165 offset:1280\n\t" \
  "s_add_u32 s66, s66, 6144\n\t" \
  "s_cmp_eq_u32 s66, 30720\n\t" \
  "s_cselect_b32 s66, 0, s66\n\t" \
  "s_add_u32 s69, s69, 1\n\t" \
  "s_add_u32 s67, s67, 6144\n\t" \
  "s_cmp_eq_u32 s67, 30720\n\t" \
  "s_cselect_b32 s67, 0, s67\n\t" \
  "s_nop 5\n\t" \
  "v_mfma_f32_32x32x16_bf16 %[a4], v[136:139], v[160:163], %[a4]\n\t" \
  "ds_read_b64_tr_b16 v[136:137], v164 offset:2048\n\t" \
  "ds_read_b64_tr_b16 v[138:139], v164 offset:2304\n\t" \
  "ds_read_b64_tr_b16 v[140:141], v165 offset:2048\n\t" \
  "ds_read_b64_tr_b16 v[142:143], v165 offset:2304\n\t" \
  "ds_read_b32 v166, %[cntr]\n\t" \
  "v_mfma_f32_32x32x16_bf16 %[a5], v[144:147], v[160:163], %[a5]\n\t" \
  "global_load_dwordx4 v[160:163], %[xo], s[64:65] offset:3072 sc1\n\t" \
  "s_nop 5\n\t" \
  "s_add_u32 s64, s64, 4096\n\t" \
  "s_addc_u32 s65, s65, 0\n\t" \
  "s_sub_u32 s70, s70, 1\n\t" \
  "s_cmp_lg_u32 s70, 0\n\t" \
  "s_cbranch_scc1 LOOP%=\n\t" \
  "s_waitcnt vmcnt(0) lgkmcnt(0)\n\t" \
  "s_setprio 3\n\t" \
  "s_nop 7\n\t" \
  "s_nop 7\n\t" \
  ""
#define FFN1_BODY_3_1 \
  "s_waitcnt lgkmcnt(0)\n\t" \
  "s_setprio 3\n\t" \
  "s_mov_b64 s[60:61], %[b0]\n\t" \
  "s_mov_b64 s[64:65], %[xb]\n\t" \
  "s_mov_b32 s66, 0\n\t" \
  "s_mov_b32 s68, 0\n\t" \
  "s_mov_b32 s69, 1\n\t" \
  "s_mov_b32 s70, 8\n\t" \
  "v_mov_b32 v167, 1\n\t" \
  "s_add_u32 m0, s66, %[m0a]\n\t" \
  "s_nop 0\n\t" \
  "global_load_lds_dwordx4 %[d0], s[60:61]\n\t" \
  "s_cmp_lt_u32 s68, 31\n\t" \
  "s_cselect_b32 s71, 44032, 0\n\t" \
  "s_addc_u32 s68, s68, 0\n\t" \
  "s_add_u32 s60, s60, s71\n\t" \
  "s_addc_u32 s61, s61, 0\n\t" \
  "s_add_u32 s66, s66, 6144\n\t" \
  "s_cmp_eq_u32 s66, 30720\n\t" \
  "s_cselect_b32 s66, 0, s66\n\t" \
  "global_load_dwordx4 v[148:151], %[xo], s[64:65] offset:0 sc1\n\t" \
  "s_add_u32 m0, s66, %[m0a]\n\t" \
  "s_nop 0\n\t" \
  "global_load_lds_dwordx4 %[d0], s[60:61]\n\t" \
  "s_cmp_lt_u32 s68, 31\n\t" \
  "s_cselect_b32 s71, 44032, 0\n\t" \
  "s_addc_u32 s68, s68, 0\n\t" \
  "s_add_u32 s60, s60, s71\n\t" \
  "s_addc_u32 s61, s61, 0\n\t" \
  "s_add_u32 s66, s66, 6144\n\t" \
  "s_cmp_eq_u32 s66, 30720\n\t" \
  "s_cselect_b32 s66, 0, s66\n\t" \
  "global_load_dwordx4 v[152:155], %[xo], s[64:65] offset:1024 sc1\n\t" \
  "s_add_u32 m0, s66, %[m0a]\n\t" \
  "s_nop 0\n\t" \
  "global_load_lds_dwordx4 %[d0], s[60:61]\n\t" \
  "s_cmp_lt_u32 s68, 31\n\t" \
  "s_cselect_b32 s71, 44032, 0\n\t" \
  "s_addc_u32 s68, s68, 0\n\t" \
  "s_add_u32 s60, s60, s71\n\t" \
  "s_addc_u32 s61, s61, 0\n\t" \
  "s_add_u32 s66, s66, 6144\n\t" \
  "s_cmp_eq_u32 s66, 30720\n\t" \
  "s_cselect_b32 s66, 0, s66\n\t" \
  "global_load_dwordx4 v[156:159], %[xo], s[64:65] offset:2048 sc1\n\t" \
  "s_add_u32 m0, s66, %[m0a]\n\t" \
  "s_nop 0\n\t" \
  "global_load_lds_dwordx4 %[d0], s[60:61]\n\t" \
  "s_cmp_lt_u32 s68, 31\n\t" \
  "s_cselect_b32 s71, 44032, 0\n\t" \
  "s_addc_u32 s68, s68, 0\n\t" \
  "s_add_u32 s60, s60, s71\n\t" \
  "s_addc_u32 s61, s61, 0\n\t" \
  "s_add_u32 s66, s66, 6144\n\t" \
  "s_cmp_eq_u32 s66, 30720\n\t" \
  "s_cselect_b32 s66, 0, s66\n\t" \
  "global_load_dwordx4 v[160:163], %[xo], s[64:65] offset:3072 sc1\n\t" \
  "s_add_u32 s64, s64, 4096\n\t" \
  "s_addc_u32 s65, s65, 0\n\t" \
  "s_waitcnt vmcnt(5)\n\t" \
  "s_mov_b64 exec, 1\n\t" \
  "ds_add_u32 %[cntw], v167\n\t" \
  "s_mov_b64 exec, -1\n\t" \
  "ds_read_b32 v166, %[cntr]\n\t" \
  "s_waitcnt lgkmcnt(0)\n\t" \
  "v_cmp_gt_u32 vcc, s69, v166\n\t" \
  "s_cbranch_vccz OKA%=\n\t" \
  "s_mov_b32 s72, 0x40000\n\t" \
  "SPA%=:\n\t" \
  "ds_read_b32 v166, %[cntr]\n\t" \
  "s_waitcnt lgkmcnt(0)\n\t" \
  "s_sub_u32 s72, s72, 1\n\t" \
  "s_cbranch_scc1 OKA%=\n\t" \
  "v_cmp_gt_u32 vcc, s69, v166\n\t" \
  "s_cbranch_vccnz SPA%=\n\t" \
  "OKA%=:\n\t" \
  "v_mov_b32 v164, %[wa0]\n\t" \
  "v_mov_b32 v165, %[wa1]\n\t" \
  "ds_read_b64_tr_b16 v[120:121], v164 offset:0\n\t" \
  "ds_read_b64_tr_b16 v[122:123], v164 offset:256\n\t" \
  "ds_read_b64_tr_b16 v[124:125], v165 offset:0\n\t" \
  "ds_read_b64_tr_b16 v[126:127], v165 offset:256\n\t" \
  "ds_read_b64_tr_b16 v[128:129], v164 offset:1024\n\t" \
  "ds_read_b64_tr_b16 v[130:131], v164 offset:1280\n\t" \
  "ds_read_b64_tr_b16 v[132:133], v165 offset:1024\n\t" \
  "ds_read_b64_tr_b16 v[134:135], v165 offset:1280\n\t" \
  "ds_read_b64_tr_b16 v[136:137], v164 offset:2048\n\t" \
  "ds_read_b64_tr_b16 v[138:139], v164 offset:2304\n\t" \
  "ds_read_b64_tr_b16 v[140:141], v165 offset:2048\n\t" \
  "ds_read_b64_tr_b16 v[142:143], v165 offset:2304\n\t" \
  "ds_read_b32 v166, %[cntr]\n\t" \
  "s_mov_b32 s67, 6144\n\t" \
  "LOOP%=:\n\t" \
  "s_waitcnt vmcnt(3) lgkmcnt(0)\n\t" \
  "v_mfma_f32_32x32x16_bf16 %[a0], v[120:123], v[148:151], %[a0]\n\t" \
  "s_mov_b64 exec, 1\n\t" \
  "ds_add_u32 %[cntw], v167\n\t" \
  "s_mov_b64 exec, -1\n\t" \
  "v_cmp_gt_u32 vcc, s69, v166\n\t" \
  "s_cbranch_vccz OKL0%=\n\t" \
  "s_mov_b32 s72, 0x40000\n\t" \
  "SPL0%=:\n\t" \
  "ds_read_b32 v166, %[cntr]\n\t" \
  "s_waitcnt lgkmcnt(0)\n\t" \
  "s_sub_u32 s72, s72, 1\n\t" \
  "s_cbranch_scc1 OKL0%=\n\t" \
  "v_cmp_gt_u32 vcc, s69, v166\n\t" \
  "s_cbranch_vccnz SPL0%=\n\t" \
  "OKL0%=:\n\t" \
  "s_add_u32 m0, s66, %[m0a]\n\t" \
  "s_nop 0\n\t" \
  "global_load_lds_dwordx4 %[d0], s[60:61]\n\t" \
  "v_add_u32 v164, s67, %[wa0]\n\t" \
  "v_add_u32 v165, s67, %[wa1]\n\t" \
  "ds_read_b64_tr_b16 v[120:121], v164 offset:0\n\t" \
  "ds_read_b64_tr_b16 v[122:123], v164 offset:256\n\t" \
  "v_mfma_f32_32x32x16_bf16 %[a1], v[124:127], v[148:151], %[a1]\n\t" \
  "ds_read_b64_tr_b16 v[124:125], v165 offset:0\n\t" \
  "ds_read_b64_tr_b16 v[126:127], v165 offset:256\n\t" \
  "s_nop 5\n\t" \
  "v_mfma_f32_32x32x16_bf16 %[a2], v[128:131], v[148:151], %[a2]\n\t" \
  "ds_read_b64_tr_b16 v[128:129], v164 offset:1024\n\t" \
  "ds_read_b64_tr_b16 v[130:131], v164 offset:1280\n\t" \
  "s_cmp_lt_u32 s68, 31\n\t" \
  "s_cselect_b32 s71, 44032, 0\n\t" \
  "s_addc_u32 s68, s68, 0\n\t" \
  "s_add_u32 s60, s60, s71\n\t" \
  "s_addc_u32 s61, s61, 0\n\t" \
  "s_add_u32 s66, s66, 6144\n\t" \
  "s_cmp_eq_u32 s66, 30720\n\t" \
  "v_mfma_f32_32x32x16_bf16 %[a3], v[132:135], v[148:151], %[a3]\n\t" \
  "ds_read_b64_tr_b16 v[132:133], v165 offset:1024\n\t" \
  "ds_read_b64_tr_b16 v[134:135], v165 offset:1280\n\t" \
  "s_cselect_b32 s66, 0, s66\n\t" \
  "s_add_u32 s69, s69, 1\n\t" \
  "s_add_u32 s67, s67, 6144\n\t" \
  "s_cmp_eq_u32 s67, 30720\n\t" \
  "s_cselect_b32 s67, 0, s67\n\t" \
  "s_nop 5\n\t" \
  "v_mfma_f32_32x32x16_bf16 %[a4], v[136:139], v[148:151], %[a4]\n\t" \
  "ds_read_b64_tr_b16 v[136:137], v164 offset:2048\n\t" \
  "ds_read_b64_tr_b16 v[138:139], v164 offset:2304\n\t" \
  "ds_read_b64_tr_b16 v[144:145], v165 offset:2048\n\t" \
  "ds_read_b64_tr_b16 v[146:147], v165 offset:2304\n\t" \
  "ds_read_b32 v166, %[cntr]\n\t" \
  "v_mfma_f32_32x32x16_bf16 %[a5], v[140:143], v[148:151], %[a5]\n\t" \
  "global_load_dwordx4 v[148:151], %[xo], s[64:65] offset:0 sc1\n\t" \
  "s_nop 5\n\t" \
  "s_waitcnt vmcnt(3) lgkmcnt(0)\n\t" \
  "v_mfma_f32_32x32x16_bf16 %[a0], v[120:123], v[152:155], %[a0]\n\t" \
  "s_mov_b64 exec, 1\n\t" \
  "ds_add_u32 %[cntw], v167\n\t" \
  "s_mov_b64 exec, -1\n\t" \
  "v_cmp_gt_u32 vcc, s69, v166\n\t" \
  "s_cbranch_vccz OKL1%=\n\t" \
  "s_mov_b32 s72, 0x40000\n\t" \
  "SPL1%=:\n\t" \
  "ds_read_b32 v166, %[cntr]\n\t" \
  "s_waitcnt lgkmcnt(0)\n\t" \
  "s_sub_u32 s72, s72, 1\n\t" \
  "s_cbranch_scc1 OKL1%=\n\t" \
  "v_cmp_gt_u32 vcc, s69, v166\n\t" \
  "s_cbranch_vccnz SPL1%=\n\t" \
  "OKL1%=:\n\t" \
  "s_add_u32 m0, s66, %[m0a]\n\t" \
  "s_nop 0\n\t" \
  "global_load_lds_dwordx4 %[d0], s[60:61]\n\t" \
  "v_add_u32 v164, s67, %[wa0]\n\t" \
  "v_add_u32 v165, s67, %[wa1]\n\t" \
  "ds_read_b64_tr_b16 v[120:121], v164 offset:0\n\t" \
  "ds_read_b64_tr_b16 v[122:123], v164 offset:256\n\t" \
  "v_mfma_f32_32x32x16_bf16 %[a1], v[124:127], v[152:155], %[a1]\n\t" \
  "ds_read_b64_tr_b16 v[124:125], v165 offset:0\n\t" \
  "ds_read_b64_tr_b16 v[126:127], v165 offset:256\n\t" \
  "s_nop 5\n\t" \
  "v_mfma_f32_32x32x16_bf16 %[a2], v[128:131], v[152:155], %[a2]\n\t" \
  "ds_read_b64_tr_b16 v[128:129], v164 offset:1024\n\t" \
  "ds_read_b64_tr_b16 v[130:131], v164 offset:1280\n\t" \
  "s_cmp_lt_u32 s68, 31\n\t" \
  "s_cselect_b32 s71, 44032, 0\n\t" \
  "s_addc_u32 s68, s68, 0\n\t" \
  "s_add_u32 s60, s60, s71\n\t" \
  "s_addc_u32 s61, s61, 0\n\t" \
  "s_add_u32 s66, s66, 6144\n\t" \
  "s_cmp_eq_u32 s66, 30720\n\t" \
  "v_mfma_f32_32x32x16_bf16 %[a3], v[132:135], v[152:155], %[a3]\n\t" \
  "ds_read_b64_tr_b16 v[132:133], v165 offset:1024\n\t" \
  "ds_read_b64_tr_b16 v[134:135], v165 offset:1280\n\t" \
  "s_cselect_b32 s66, 0, s66\n\t" \
  "s_add_u32 s69, s69, 1\n\t" \
  "s_add_u32 s67, s67, 6144\n\t" \
  "s_cmp_eq_u32 s67, 30720\n\t" \
  "s_cselect_b32 s67, 0, s67\n\t" \
  "s_nop 5\n\t" \
  "v_mfma_f32_32x32x16_bf16 %[a4], v[136:139], v[152:155], %[a4]\n\t" \
  "ds_read_b64_tr_b16 v[136:137], v164 offset:2048\n\t" \
  "ds_read_b64_tr_b16 v[138:139], v164 offset:2304\n\t" \
  "ds_read_b64_tr_b16 v[140:141], v165 offset:2048\n\t" \
  "ds_read_b64_tr_b16 v[142:143], v165 offset:2304\n\t" \
  "ds_read_b32 v166, %[cntr]\n\t" \
  "v_mfma_f32_32x32x16_bf16 %[a5], v[144:147], v[152:155], %[a5]\n\t" \
  "global_load_dwordx4 v[152:155], %[xo], s[64:65] offset:1024 sc1\n\t" \
  "s_nop 5\n\t" \
  "s_waitcnt vmcnt(3) lgkmcnt(0)\n\t" \
  "v_mfma_f32_32x32x16_bf16 %[a0], v[120:123], v[156:159], %[a0]\n\t" \
  "s_mov_b64 exec, 1\n\t" \
  "ds_add_u32 %[cntw], v167\n\t" \
  "s_mov_b64 exec, -1\n\t" \
  "v_cmp_gt_u32 vcc, s69, v166\n\t" \
  "s_cbranch_vccz OKL2%=\n\t" \
  "s_mov_b32 s72, 0x40000\n\t" \
  "SPL2%=:\n\t" \
  "ds_read_b32 v166, %[cntr]\n\t" \
  "s_waitcnt lgkmcnt(0)\n\t" \
  "s_sub_u32 s72, s72, 1\n\t" \
  "s_cbranch_scc1 OKL2%=\n\t" \
  "v_cmp_gt_u32 vcc, s69, v166\n\t" \
  "s_cbranch_vccnz SPL2%=\n\t" \
  "OKL2%=:\n\t" \
  "s_add_u32 m0, s66, %[m0a]\n\t" \
  "s_nop 0\n\t" \
  "global_load_lds_dwordx4 %[d0], s[60:61]\n\t" \
  "v_add_u32 v164, s67, %[wa0]\n\t" \
  "v_add_u32 v165, s67, %[wa1]\n\t" \
  "ds_read_b64_tr_b16 v[120:121], v164 offset:0\n\t" \
  "ds_read_b64_tr_b16 v[122:123], v164 offset:256\n\t" \
  "v_mfma_f32_32x32x16_bf16 %[a1], v[124:127], v[156:159], %[a1]\n\t" \
  "ds_read_b64_tr_b16 v[124:125], v165 offset:0\n\t" \
  "ds_read_b64_tr_b16 v[126:127], v165 offset:256\n\t" \
  "s_nop 5\n\t" \
  "v_mfma_f32_32x32x16_bf16 %[a2], v[128:131], v[156:159], %[a2]\n\t" \
  "ds_read_b64_tr_b16 v[128:129], v164 offset:1024\n\t" \
  "ds_read_b64_tr_b16 v[130:131], v164 offset:1280\n\t" \
  "s_cmp_lt_u32 s68, 31\n\t" \
  "s_cselect_b32 s71, 44032, 0\n\t" \
  "s_addc_u32 s68, s68, 0\n\t" \
  "s_add_u32 s60, s60, s71\n\t" \
  "s_addc_u32 s61, s61, 0\n\t" \
  "s_add_u32 s66, s66, 6144\n\t" \
  "s_cmp_eq_u32 s66, 30720\n\t" \
  "v_mfma_f32_32x32x16_bf16 %[a3], v[132:135], v[156:159], %[a3]\n\t" \
  "ds_read_b64_tr_b16 v[132:133], v165 offset:1024\n\t" \
  "ds_read_b64_tr_b16 v[134:135], v165 offset:1280\n\t" \
  "s_cselect_b32 s66, 0, s66\n\t" \
  "s_add_u32 s69, s69, 1\n\t" \
  "s_add_u32 s67, s67, 6144\n\t" \
  "s_cmp_eq_u32 s67, 30720\n\t" \
  "s_cselect_b32 s67, 0, s67\n\t" \
  "s_nop 5\n\t" \
  "v_mfma_f32_32x32x16_bf16 %[a4], v[136:139], v[156:159], %[a4]\n\t" \
  "ds_read_b64_tr_b16 v[136:137], v164 offset:2048\n\t" \
  "ds_read_b64_tr_b16 v[138:139], v164 offset:2304\n\t" \
  "ds_read_b64_tr_b16 v[144:145], v165 offset:2048\n\t" \
  "ds_read_b64_tr_b16 v[146:147], v165 offset:2304\n\t" \
  "ds_read_b32 v166, %[cntr]\n\t" \
  "v_mfma_f32_32x32x16_bf16 %[a5], v[140:143], v[156:159], %[a5]\n\t" \
  "global_load_dwordx4 v[156:159], %[xo], s[64:65] offset:2048 sc1\n\t" \
  "s_nop 5\n\t" \
  "s_waitcnt vmcnt(3) lgkmcnt(0)\n\t" \
  "v_mfma_f32_32x32x16_bf16 %[a0], v[120:123], v[160:163], %[a0]\n\t" \
  "s_mov_b64 exec, 1\n\t" \
  "ds_add_u32 %[cntw], v167\n\t" \
  "s_mov_b64 exec, -1\n\t" \
  "v_cmp_gt_u32 vcc, s69, v166\n\t" \
  "s_cbranch_vccz OKL3%=\n\t" \
  "s_mov_b32 s72, 0x40000\n\t" \
  "SPL3%=:\n\t" \
  "ds_read_b32 v166, %[cntr]\n\t" \
  "s_waitcnt lgkmcnt(0)\n\t" \
  "s_sub_u32 s72, s72, 1\n\t" \
  "s_cbranch_scc1 OKL3%=\n\t" \
  "v_cmp_gt_u32 vcc, s69, v166\n\t" \
  "s_cbranch_vccnz SPL3%=\n\t" \
  "OKL3%=:\n\t" \
  "s_add_u32 m0, s66, %[m0a]\n\t" \
  "s_nop 0\n\t" \
  "global_load_lds_dwordx4 %[d0], s[60:61]\n\t" \
  "v_add_u32 v164, s67, %[wa0]\n\t" \
  "v_add_u32 v165, s67, %[wa1]\n\t" \
  "ds_read_b64_tr_b16 v[120:121], v164 offset:0\n\t" \
  "ds_read_b64_tr_b16 v[122:123], v164 offset:256\n\t" \
  "v_mfma_f32_32x32x16_bf16 %[a1], v[124:127], v[160:163], %[a1]\n\t" \
  "ds_read_b64_tr_b16 v[124:125], v165 offset:0\n\t" \
  "ds_read_b64_tr_b16 v[126:127], v165 offset:256\n\t" \
  "s_nop 5\n\t" \
  "v_mfma_f32_32x32x16_bf16 %[a2], v[128:131], v[160:163], %[a2]\n\t" \
  "ds_read_b64_tr_b16 v[128:129], v164 offset:1024\n\t" \
  "ds_read_b64_tr_b16 v[130:131], v164 offset:1280\n\t" \
  "s_cmp_lt_u32 s68, 31\n\t" \
  "s_cselect_b32 s71, 44032, 0\n\t" \
  "s_addc_u32 s68, s68, 0\n\t" \
  "s_add_u32 s60, s60, s71\n\t" \
  "s_addc_u32 s61, s61, 0\n\t" \
  "s_add_u32 s66, s66, 6144\n\t" \
  "s_cmp_eq_u32 s66, 30720\n\t" \
  "v_mfma_f32_32x32x16_bf16 %[a3], v[132:135], v[160:163], %[a3]\n\t" \
  "ds_read_b64_tr_b16 v[132:133], v165 offset:1024\n\t" \
  "ds_read_b64_tr_b16 v[134:135], v165 offset:1280\n\t" \
  "s_cselect_b32 s66, 0, s66\n\t" \
  "s_add_u32 s69, s69, 1\n\t" \
  "s_add_u32 s67, s67, 6144\n\t" \
  "s_cmp_eq_u32 s67, 30720\n\t" \
  "s_cselect_b32 s67, 0, s67\n\t" \
  "s_nop 5\n\t" \
  "v_mfma_f32_32x32x16_bf16 %[a4], v[136:139], v[160:163], %[a4]\n\t" \
  "ds_read_b64_tr_b16 v[136:137], v164 offset:2048\n\t" \
  "ds_read_b64_tr_b16 v[138:139], v164 offset:2304\n\t" \
  "ds_read_b64_tr_b16 v[140:141], v165 offset:2048\n\t" \
  "ds_read_b64_tr_b16 v[142:143], v165 offset:2304\n\t" \
  "ds_read_b32 v166, %[cntr]\n\t" \
  "v_mfma_f32_32x32x16_bf16 %[a5], v[144:147], v[160:163], %[a5]\n\t" \
  "global_load_dwordx4 v[160:163], %[xo], s[64:65] offset:3072 sc1\n\t" \
  "s_nop 5\n\t" \
  "s_add_u32 s64, s64, 4096\n\t" \
  "s_addc_u32 s65, s65, 0\n\t" \
  "s_sub_u32 s70, s70, 1\n\t" \
  "s_cmp_lg_u32 s70, 0\n\t" \
  "s_cbranch_scc1 LOOP%=\n\t" \
  "s_waitcnt vmcnt(0) lgkmcnt(0)\n\t" \
  "s_setprio 3\n\t" \
  "s_nop 7\n\t" \
  "s_nop 7\n\t" \
  ""
#define FFN1_BODY_2_1 \
  "s_waitcnt lgkmcnt(0)\n\t" \
  "s_setprio 3\n\t" \
  "s_mov_b64 s[60:61], %[b0]\n\t" \
  "s_mov_b64 s[64:65], %[xb]\n\t" \
  "s_mov_b32 s66, 0\n\t" \
  "s_mov_b32 s68, 0\n\t" \
  "s_mov_b32 s69, 1\n\t" \
  "s_mov_b32 s70, 8\n\t" \
  "v_mov_b32 v167, 1\n\t" \
  "s_add_u32 m0, s66, %[m0a]\n\t" \
  "s_nop 0\n\t" \
  "global_load_lds_dwordx4 %[d0], s[60:61]\n\t" \
  "s_cmp_lt_u32 s68, 31\n\t" \
  "s_cselect_b32 s71, 44032, 0\n\t" \
  "s_addc_u32 s68, s68, 0\n\t" \
  "s_add_u32 s60, s60, s71\n\t" \
  "s_addc_u32 s61, s61, 0\n\t" \
  "s_add_u32 s66, s66, 6144\n\t" \
  "s_cmp_eq_u32 s66, 30720\n\t" \
  "s_cselect_b32 s66, 0, s66\n\t" \
  "global_load_dwordx4 v[148:151], %[xo], s[64:65] offset:0 sc1\n\t" \
  "s_add_u32 m0, s66, %[m0a]\n\t" \
  "s_nop 0\n\t" \
  "global_load_lds_dwordx4 %[d0], s[60:61]\n\t" \
  "s_cmp_lt_u32 s68, 31\n\t" \
  "s_cselect_b32 s71, 44032, 0\n\t" \
  "s_addc_u32 s68, s68, 0\n\t" \
  "s_add_u32 s60, s60, s71\n\t" \
  "s_addc_u32 s61, s61, 0\n\t" \
  "s_add_u32 s66, s66, 6144\n\t" \
  "s_cmp_eq_u32 s66, 30720\n\t" \
  "s_cselect_b32 s66, 0, s66\n\t" \
  "global_load_dwordx4 v[152:155], %[xo], s[64:65] offset:1024 sc1\n\t" \
  "s_add_u32 m0, s66, %[m0a]\n\t" \
  "s_nop 0\n\t" \
  "global_load_lds_dwordx4 %[d0], s[60:61]\n\t" \
  "s_cmp_lt_u32 s68, 31\n\t" \
  "s_cselect_b32 s71, 44032, 0\n\t" \
  "s_addc_u32 s68, s68, 0\n\t" \
  "s_add_u32 s60, s60, s71\n\t" \
  "s_addc_u32 s61, s61, 0\n\t" \
  "s_add_u32 s66, s66, 6144\n\t" \
  "s_cmp_eq_u32 s66, 30720\n\t" \
  "s_cselect_b32 s66, 0, s66\n\t" \
  "global_load_dwordx4 v[156:159], %[xo], s[64:65] offset:2048 sc1\n\t" \
  "s_add_u32 m0, s66, %[m0a]\n\t" \
  "s_nop 0\n\t" \
  "global_load_lds_dwordx4 %[d0], s[60:61]\n\t" \
  "s_cmp_lt_u32 s68, 31\n\t" \
  "s_cselect_b32 s71, 44032, 0\n\t" \
  "s_addc_u32 s68, s68, 0\n\t" \
  "s_add_u32 s60, s60, s71\n\t" \
  "s_addc_u32 s61, s61, 0\n\t" \
  "s_add_u32 s66, s66, 6144\n\t" \
  "s_cmp_eq_u32 s66, 30720\n\t" \
  "s_cselect_b32 s66, 0, s66\n\t" \
  "global_load_dwordx4 v[160:163], %[xo], s[64:65] offset:3072 sc1\n\t" \
  "s_add_u32 s64, s64, 4096\n\t" \
  "s_addc_u32 s65, s65, 0\n\t" \
  "s_waitcnt vmcnt(5)\n\t" \
  "s_mov_b64 exec, 1\n\t" \
  "ds_add_u32 %[cntw], v167\n\t" \
  "s_mov_b64 exec, -1\n\t" \
  "ds_read_b32 v166, %[cntr]\n\t" \
  "s_waitcnt lgkmcnt(0)\n\t" \
  "v_cmp_gt_u32 vcc, s69, v166\n\t" \
  "s_cbranch_vccz OKA%=\n\t" \
  "s_mov_b32 s72, 0x40000\n\t" \
  "SPA%=:\n\t" \
  "ds_read_b32 v166, %[cntr]\n\t" \
  "s_waitcnt lgkmcnt(0)\n\t" \
  "s_sub_u32 s72, s72, 1\n\t" \
  "s_cbranch_scc1 OKA%=\n\t" \
  "v_cmp_gt_u32 vcc, s69, v166\n\t" \
  "s_cbranch_vccnz SPA%=\n\t" \
  "OKA%=:\n\t" \
  "v_mov_b32 v164, %[wa0]\n\t" \
  "v_mov_b32 v165, %[wa1]\n\t" \
  "ds_read_b64_tr_b16 v[120:121], v164 offset:0\n\t" \
  "ds_read_b64_tr_b16 v[122:123], v164 offset:256\n\t" \
  "ds_read_b64_tr_b16 v[124:125], v165 offset:0\n\t" \
  "ds_read_b64_tr_b16 v[126:127], v165 offset:256\n\t" \
  "ds_read_b64_tr_b16 v[128:129], v164 offset:1024\n\t" \
  "ds_read_b64_tr_b16 v[130:131], v164 offset:1280\n\t" \
  "ds_read_b64_tr_b16 v[132:133], v165 offset:1024\n\t" \
  "ds_read_b64_tr_b16 v[134:135], v165 offset:1280\n\t" \
  "ds_read_b32 v166, %[cntr]\n\t" \
  "s_mov_b32 s67, 6144\n\t" \
  "LOOP%=:\n\t" \
  "s_waitcnt vmcnt(3) lgkmcnt(0)\n\t" \
  "v_mfma_f32_32x32x16_bf16 %[a0], v[120:123], v[148:151], %[a0]\n\t" \
  "s_mov_b64 exec, 1\n\t" \
  "ds_add_u32 %[cntw], v167\n\t" \
  "s_mov_b64 exec, -1\n\t" \
  "v_cmp_gt_u32 vcc, s69, v166\n\t" \
  "s_cbranch_vccz OKL0%=\n\t" \
  "s_mov_b32 s72, 0x40000\n\t" \
  "SPL0%=:\n\t" \
  "ds_read_b32 v166, %[cntr]\n\t" \
  "s_waitcnt lgkmcnt(0)\n\t" \
  "s_sub_u32 s72, s72, 1\n\t" \
  "s_cbranch_scc1 OKL0%=\n\t" \
  "v_cmp_gt_u32 vcc, s69, v166\n\t" \
  "s_cbranch_vccnz SPL0%=\n\t" \
  "OKL0%=:\n\t" \
  "s_add_u32 m0, s66, %[m0a]\n\t" \
  "s_nop 0\n\t" \
  "global_load_lds_dwordx4 %[d0], s[60:61]\n\t" \
  "v_add_u32 v164, s67, %[wa0]\n\t" \
  "v_add_u32 v165, s67, %[wa1]\n\t" \
  "ds_read_b64_tr_b16 v[120:121], v164 offset:0\n\t" \
  "ds_read_b64_tr_b16 v[122:123], v164 offset:256\n\t" \
  "v_mfma_f32_32x32x16_bf16 %[a1], v[124:127], v[148:151], %[a1]\n\t" \
  "ds_read_b64_tr_b16 v[124:125], v165 offset:0\n\t" \
  "ds_read_b64_tr_b16 v[126:127], v165 offset:256\n\t" \
  "s_nop 5\n\t" \
  "v_mfma_f32_32x32x16_bf16 %[a2], v[128:131], v[148:151], %[a2]\n\t" \
  "ds_read_b64_tr_b16 v[128:129], v164 offset:1024\n\t" \
  "ds_read_b64_tr_b16 v[130:131], v164 offset:1280\n\t" \
  "s_cmp_lt_u32 s68, 31\n\t" \
  "s_cselect_b32 s71, 44032, 0\n\t" \
  "s_addc_u32 s68, s68, 0\n\t" \
  "s_add_u32 s60, s60, s71\n\t" \
  "s_addc_u32 s61, s61, 0\n\t" \
  "s_add_u32 s66, s66, 6144\n\t" \
  "s_cmp_eq_u32 s66, 30720\n\t" \
  "s_cselect_b32 s66, 0, s66\n\t" \
  "s_add_u32 s69, s69, 1\n\t" \
  "s_add_u32 s67, s67, 6144\n\t" \
  "s_cmp_eq_u32 s67, 30720\n\t" \
  "s_cselect_b32 s67, 0, s67\n\t" \
  "ds_read_b64_tr_b16 v[144:145], v165 offset:1024\n\t" \
  "ds_read_b64_tr_b16 v[146:147], v165 offset:1280\n\t" \
  "ds_read_b32 v166, %[cntr]\n\t" \
  "v_mfma_f32_32x32x16_bf16 %[a3], v[132:135], v[148:151], %[a3]\n\t" \
  "global_load_dwordx4 v[148:151], %[xo], s[64:65] offset:0 sc1\n\t" \
  "s_nop 5\n\t" \
  "s_waitcnt vmcnt(3) lgkmcnt(0)\n\t" \
  "v_mfma_f32_32x32x16_bf16 %[a0], v[120:123], v[152:155], %[a0]\n\t" \
  "s_mov_b64 exec, 1\n\t" \
  "ds_add_u32 %[cntw], v167\n\t" \
  "s_mov_b64 exec, -1\n\t" \
  "v_cmp_gt_u32 vcc, s69, v166\n\t" \
  "s_cbranch_vccz OKL1%=\n\t" \
  "s_mov_b32 s72, 0x40000\n\t" \
  "SPL1%=:\n\t" \
  "ds_read_b32 v166, %[cntr]\n\t" \
  "s_waitcnt lgkmcnt(0)\n\t" \
  "s_sub_u32 s72, s72, 1\n\t" \
  "s_cbranch_scc1 OKL1%=\n\t" \
  "v_cmp_gt_u32 vcc, s69, v166\n\t" \
  "s_cbranch_vccnz SPL1%=\n\t" \
  "OKL1%=:\n\t" \
  "s_add_u32 m0, s66, %[m0a]\n\t" \
  "s_nop 0\n\t" \
  "global_load_lds_dwordx4 %[d0], s[60:61]\n\t" \
  "v_add_u32 v164, s67, %[wa0]\n\t" \
  "v_add_u32 v165, s67, %[wa1]\n\t" \
  "ds_read_b64_tr_b16 v[120:121], v164 offset:0\n\t" \
  "ds_read_b64_tr_b16 v[122:123], v164 offset:256\n\t" \
  "v_mfma_f32_32x32x16_bf16 %[a1], v[124:127], v[152:155], %[a1]\n\t" \
  "ds_read_b64_tr_b16 v[124:125], v165 offset:0\n\t" \
  "ds_read_b64_tr_b16 v[126:127], v165 offset:256\n\t" \
  "s_nop 5\n\t" \
  "v_mfma_f32_32x32x16_bf16 %[a2], v[128:131], v[152:155], %[a2]\n\t" \
  "ds_read_b64_tr_b16 v[128:129], v164 offset:1024\n\t" \
  "ds_read_b64_tr_b16 v[130:131], v164 offset:1280\n\t" \
  "s_cmp_lt_u32 s68, 31\n\t" \
  "s_cselect_b32 s71, 44032, 0\n\t" \
  "s_addc_u32 s68, s68, 0\n\t" \
  "s_add_u32 s60, s60, s71\n\t" \
  "s_addc_u32 s61, s61, 0\n\t" \
  "s_add_u32 s66, s66, 6144\n\t" \
  "s_cmp_eq_u32 s66, 30720\n\t" \
  "s_cselect_b32 s66, 0, s66\n\t" \
  "s_add_u32 s69, s69, 1\n\t" \
  "s_add_u32 s67, s67, 6144\n\t" \
  "s_cmp_eq_u32 s67, 30720\n\t" \
  "s_cselect_b32 s67, 0, s67\n\t" \
  "ds_read_b64_tr_b16 v[132:133], v165 offset:1024\n\t" \
  "ds_read_b64_tr_b16 v[134:135], v165 offset:1280\n\t" \
  "ds_read_b32 v166, %[cntr]\n\t" \
  "v_mfma_f32_32x32x16_bf16 %[a3], v[144:147], v[152:155], %[a3]\n\t" \
  "global_load_dwordx4 v[152:155], %[xo], s[64:65] offset:1024 sc1\n\t" \
  "s_nop 5\n\t" \
  "s_waitcnt vmcnt(3) lgkmcnt(0)\n\t" \
  "v_mfma_f32_32x32x16_bf16 %[a0], v[120:123], v[156:159], %[a0]\n\t" \
  "s_mov_b64 exec, 1\n\t" \
  "ds_add_u32 %[cntw], v167\n\t" \
  "s_mov_b64 exec, -1\n\t" \
  "v_cmp_gt_u32 vcc, s69, v166\n\t" \
  "s_cbranch_vccz OKL2%=\n\t" \
  "s_mov_b32 s72, 0x40000\n\t" \
  "SPL2%=:\n\t" \
  "ds_read_b32 v166, %[cntr]\n\t" \
  "s_waitcnt lgkmcnt(0)\n\t" \
  "s_sub_u32 s72, s72, 1\n\t" \
  "s_cbranch_scc1 OKL2%=\n\t" \
  "v_cmp_gt_u32 vcc, s69, v166\n\t" \
  "s_cbranch_vccnz SPL2%=\n\t" \
  "OKL2%=:\n\t" \
  "s_add_u32 m0, s66, %[m0a]\n\t" \
  "s_nop 0\n\t" \
  "global_load_lds_dwordx4 %[d0], s[60:61]\n\t" \
  "v_add_u32 v164, s67, %[wa0]\n\t" \
  "v_add_u32 v165, s67, %[wa1]\n\t" \
  "ds_read_b64_tr_b16 v[120:121], v164 offset:0\n\t" \
  "ds_read_b64_tr_b16 v[122:123], v164 offset:256\n\t" \
  "v_mfma_f32_32x32x16_bf16 %[a1], v[124:127], v[156:159], %[a1]\n\t" \
  "ds_read_b64_tr_b16 v[124:125], v165 offset:0\n\t" \
  "ds_read_b64_tr_b16 v[126:127], v165 offset:256\n\t" \
  "s_nop 5\n\t" \
  "v_mfma_f32_32x32x16_bf16 %[a2], v[128:131], v[156:159], %[a2]\n\t" \
  "ds_read_b64_tr_b16 v[128:129], v164 offset:1024\n\t" \
  "ds_read_b64_tr_b16 v[130:131], v164 offset:1280\n\t" \
  "s_cmp_lt_u32 s68, 31\n\t" \
  "s_cselect_b32 s71, 44032, 0\n\t" \
  "s_addc_u32 s68, s68, 0\n\t" \
  "s_add_u32 s60, s60, s71\n\t" \
  "s_addc_u32 s61, s61, 0\n\t" \
  "s_add_u32 s66, s66, 6144\n\t" \
  "s_cmp_eq_u32 s66, 30720\n\t" \
  "s_cselect_b32 s66, 0, s66\n\t" \
  "s_add_u32 s69, s69, 1\n\t" \
  "s_add_u32 s67, s67, 6144\n\t" \
  "s_cmp_eq_u32 s67, 30720\n\t" \
  "s_cselect_b32 s67, 0, s67\n\t" \
  "ds_read_b64_tr_b16 v[144:145], v165 offset:1024\n\t" \
  "ds_read_b64_tr_b16 v[146:147], v165 offset:1280\n\t" \
  "ds_read_b32 v166, %[cntr]\n\t" \
  "v_mfma_f32_32x32x16_bf16 %[a3], v[132:135], v[156:159], %[a3]\n\t" \
  "global_load_dwordx4 v[156:159], %[xo], s[64:65] offset:2048 sc1\n\t" \
  "s_nop 5\n\t" \
  "s_waitcnt vmcnt(3) lgkmcnt(0)\n\t" \
  "v_mfma_f32_32x32x16_bf16 %[a0], v[120:123], v[160:163], %[a0]\n\t" \
  "s_mov_b64 exec, 1\n\t" \
  "ds_add_u32 %[cntw], v167\n\t" \
  "s_mov_b64 exec, -1\n\t" \
  "v_cmp_gt_u32 vcc, s69, v166\n\t" \
  "s_cbranch_vccz OKL3%=\n\t" \
  "s_mov_b32 s72, 0x40000\n\t" \
  "SPL3%=:\n\t" \
  "ds_read_b32 v166, %[cntr]\n\t" \
  "s_waitcnt lgkmcnt(0)\n\t" \
  "s_sub_u32 s72, s72, 1\n\t" \
  "s_cbranch_scc1 OKL3%=\n\t" \
  "v_cmp_gt_u32 vcc, s69, v166\n\t" \
  "s_cbranch_vccnz SPL3%=\n\t" \
  "OKL3%=:\n\t" \
  "s_add_u32 m0, s66, %[m0a]\n\t" \
  "s_nop 0\n\t" \
  "global_load_lds_dwordx4 %[d0], s[60:61]\n\t" \
  "v_add_u32 v164, s67, %[wa0]\n\t" \
  "v_add_u32 v165, s67, %[wa1]\n\t" \
  "ds_read_b64_tr_b16 v[120:121], v164 offset:0\n\t" \
  "ds_read_b64_tr_b16 v[122:123], v164 offset:256\n\t" \
  "v_mfma_f32_32x32x16_bf16 %[a1], v[124:127], v[160:163], %[a1]\n\t" \
  "ds_read_b64_tr_b16 v[124:125], v165 offset:0\n\t" \
  "ds_read_b64_tr_b16 v[126:127], v165 offset:256\n\t" \
  "s_nop 5\n\t" \
  "v_mfma_f32_32x32x16_bf16 %[a2], v[128:131], v[160:163], %[a2]\n\t" \
  "ds_read_b64_tr_b16 v[128:129], v164 offset:1024\n\t" \
  "ds_read_b64_tr_b16 v[130:131], v164 offset:1280\n\t" \
  "s_cmp_lt_u32 s68, 31\n\t" \
  "s_cselect_b32 s71, 44032, 0\n\t" \
  "s_addc_u32 s68, s68, 0\n\t" \
  "s_add_u32 s60, s60, s71\n\t" \
  "s_addc_u32 s61, s61, 0\n\t" \
  "s_add_u32 s66, s66, 6144\n\t" \
  "s_cmp_eq_u32 s66, 30720\n\t" \
  "s_cselect_b32 s66, 0, s66\n\t" \
  "s_add_u32 s69, s69, 1\n\t" \
  "s_add_u32 s67, s67, 6144\n\t" \
  "s_cmp_eq_u32 s67, 30720\n\t" \
  "s_cselect_b32 s67, 0, s67\n\t" \
  "ds_read_b64_tr_b16 v[132:133], v165 offset:1024\n\t" \
  "ds_read_b64_tr_b16 v[134:135], v165 offset:1280\n\t" \
  "ds_read_b32 v166, %[cntr]\n\t" \
  "v_mfma_f32_32x32x16_bf16 %[a3], v[144:147], v[160:163], %[a3]\n\t" \
  "global_load_dwordx4 v[160:163], %[xo], s[64:65] offset:3072 sc1\n\t" \
  "s_nop 5\n\t" \
  "s_add_u32 s64, s64, 4096\n\t" \
  "s_addc_u32 s65, s65, 0\n\t" \
  "s_sub_u32 s70, s70, 1\n\t" \
  "s_cmp_lg_u32 s70, 0\n\t" \
  "s_cbranch_scc1 LOOP%=\n\t" \
  "s_waitcnt vmcnt(0) lgkmcnt(0)\n\t" \
  "s_setprio 3\n\t" \
  "s_nop 7\n\t" \
  "s_nop 7\n\t" \
  ""
#define FFN1_CLOB "v120","v121","v122","v123","v124","v125","v126","v127","v128","v129","v130","v131","v132","v133","v134","v135","v136","v137","v138","v139","v140","v141","v142","v143","v144","v145","v146","v147","v148","v149","v150","v151","v152","v153","v154","v155","v156","v157","v158","v159","v160","v161","v162","v163","v164","v165","v166","v167","s60","s61","s62","s63","s64","s65","s66","s67","s68","s69","s70","s71","s72","vcc"
#define P1_BODY_1_0 \
  "s_waitcnt lgkmcnt(0)\n\t" \
  "v_mov_b32 v156, 0x3f803f80\n\t" \
  "v_mov_b32 v157, 0x3f803f80\n\t" \
  "v_mov_b32 v158, 0x3f803f80\n\t" \
  "v_mov_b32 v159, 0x3f803f80\n\t" \
  "ds_read_b128 v[96:99], %[xa0]\n\t" \
  "ds_read_b64_tr_b16 v[116:117], %[wa0] offset:0\n\t" \
  "ds_read_b64_tr_b16 v[118:119], %[wa0] offset:512\n\t" \
  "ds_read_b128 v[100:103], %[xa1]\n\t" \
  "ds_read_b64_tr_b16 v[120:121], %[wa0] offset:2048\n\t" \
  "ds_read_b64_tr_b16 v[122:123], %[wa0] offset:2560\n\t" \
  "ds_read_b128 v[104:107], %[xa2]\n\t" \
  "ds_read_b64_tr_b16 v[124:125], %[wa0] offset:4096\n\t" \
  "ds_read_b64_tr_b16 v[126:127], %[wa0] offset:4608\n\t" \
  "ds_read_b128 v[108:111], %[xa3]\n\t" \
  "ds_read_b64_tr_b16 v[128:129], %[wa0] offset:6144\n\t" \
  "ds_read_b64_tr_b16 v[130:131], %[wa0] offset:6656\n\t" \
  "s_mov_b32 s60, 0\n\t" \
  "s_mov_b32 s62, 3\n\t" \
  "s_mov_b32 s63, 0xfffe8000\n\t" \
  "LOOP%=:\n\t" \
  "s_waitcnt lgkmcnt(0)\n\t" \
  "s_barrier\n\t" \
  "s_cmp_eq_u32 s60, 3\n\t" \
  "s_cselect_b32 s61, s63, 0x8000\n\t" \
  "s_add_u32 s60, s60, 1\n\t" \
  "s_and_b32 s60, s60, 3\n\t" \
  "v_add_u32 %[xa0], s61, %[xa0]\n\t" \
  "v_add_u32 %[xa1], s61, %[xa1]\n\t" \
  "v_add_u32 %[xa2], s61, %[xa2]\n\t" \
  "v_add_u32 %[xa3], s61, %[xa3]\n\t" \
  "v_add_u32 %[wa0], s61, %[wa0]\n\t" \
  "v_mfma_f32_32x32x16_bf16 %[a0], v[116:119], v[96:99], %[a0]\n\t" \
  "v_mfma_f32_32x32x16_bf16 %[c0], v[116:119], v[156:159], %[c0]\n\t" \
  "ds_read_b128 v[96:99], %[xa0]\n\t" \
  "ds_read_b64_tr_b16 v[116:117], %[wa0] offset:0\n\t" \
  "ds_read_b64_tr_b16 v[118:119], %[wa0] offset:512\n\t" \
  "v_mfma_f32_32x32x16_bf16 %[a0], v[120:123], v[100:103], %[a0]\n\t" \
  "v_mfma_f32_32x32x16_bf16 %[c0], v[120:123], v[156:159], %[c0]\n\t" \
  "ds_read_b128 v[100:103], %[xa1]\n\t" \
  "ds_read_b64_tr_b16 v[120:121], %[wa0] offset:2048\n\t" \
  "ds_read_b64_tr_b16 v[122:123], %[wa0] offset:2560\n\t" \
  "v_mfma_f32_32x32x16_bf16 %[a0], v[124:127], v[104:107], %[a0]\n\t" \
  "ds_read_b128 v[104:107], %[xa2]\n\t" \
  "ds_read_b64_tr_b16 v[124:125], %[wa0] offset:4096\n\t" \
  "ds_read_b64_tr_b16 v[126:127], %[wa0] offset:4608\n\t" \
  "ds_read_b128 v[112:115], %[xa3]\n\t" \
  "ds_read_b64_tr_b16 v[148:149], %[wa0] offset:6144\n\t" \
  "ds_read_b64_tr_b16 v[150:151], %[wa0] offset:6656\n\t" \
  "v_mfma_f32_32x32x16_bf16 %[a0], v[128:131], v[108:111], %[a0]\n\t" \
  "s_waitcnt lgkmcnt(0)\n\t" \
  "s_barrier\n\t" \
  "s_cmp_eq_u32 s60, 3\n\t" \
  "s_cselect_b32 s61, s63, 0x8000\n\t" \
  "s_add_u32 s60, s60, 1\n\t" \
  "s_and_b32 s60, s60, 3\n\t" \
  "v_add_u32 %[xa0], s61, %[xa0]\n\t" \
  "v_add_u32 %[xa1], s61, %[xa1]\n\t" \
  "v_add_u32 %[xa2], s61, %[xa2]\n\t" \
  "v_add_u32 %[xa3], s61, %[xa3]\n\t" \
  "v_add_u32 %[wa0], s61, %[wa0]\n\t" \
  "v_mfma_f32_32x32x16_bf16 %[a0], v[116:119], v[96:99], %[a0]\n\t" \
  "v_mfma_f32_32x32x16_bf16 %[c0], v[116:119], v[156:159], %[c0]\n\t" \
  "ds_read_b128 v[96:99], %[xa0]\n\t" \
  "ds_read_b64_tr_b16 v[116:117], %[wa0] offset:0\n\t" \
  "ds_read_b64_tr_b16 v[118:119], %[wa0] offset:512\n\t" \
  "v_mfma_f32_32x32x16_bf16 %[a0], v[120:123], v[100:103], %[a0]\n\t" \
  "v_mfma_f32_32x32x16_bf16 %[c0], v[120:123], v[156:159], %[c0]\n\t" \
  "ds_read_b128 v[100:103], %[xa1]\n\t" \
  "ds_read_b64_tr_b16 v[120:121], %[wa0] offset:2048\n\t" \
  "ds_read_b64_tr_b16 v[122:123], %[wa0] offset:2560\n\t" \
  "v_mfma_f32_32x32x16_bf16 %[a0], v[124:127], v[104:107], %[a0]\n\t" \
  "ds_read_b128 v[104:107], %[xa2]\n\t" \
  "ds_read_b64_tr_b16 v[124:125], %[wa0] offset:4096\n\t" \
  "ds_read_b64_tr_b16 v[126:127], %[wa0] offset:4608\n\t" \
  "ds_read_b128 v[108:111], %[xa3]\n\t" \
  "ds_read_b64_tr_b16 v[128:129], %[wa0] offset:6144\n\t" \
  "ds_read_b64_tr_b16 v[130:131], %[wa0] offset:6656\n\t" \
  "v_mfma_f32_32x32x16_bf16 %[a0], v[148:151], v[112:115], %[a0]\n\t" \
  "s_cmp_eq_u32 s62, 2\n\t" \
  "s_cbranch_scc0 NF%=\n\t" \
  "s_waitcnt vmcnt(0)\n\t" \
  "s_mov_b64 exec, 1\n\t" \
  "global_store_dwordx2 %[fxo], %[tag], %[ws] sc1\n\t" \
  "s_mov_b64 exec, -1\n\t" \
  "NF%=:\n\t" \
  "s_sub_u32 s62, s62, 1\n\t" \
  "s_cmp_lg_u32 s62, 0\n\t" \
  "s_cbranch_scc1 LOOP%=\n\t" \
  "s_waitcnt lgkmcnt(0)\n\t" \
  "s_barrier\n\t" \
  "s_cmp_eq_u32 s60, 3\n\t" \
  "s_cselect_b32 s61, s63, 0x8000\n\t" \
  "s_add_u32 s60, s60, 1\n\t" \
  "s_and_b32 s60, s60, 3\n\t" \
  "v_add_u32 %[xa0], s61, %[xa0]\n\t" \
  "v_add_u32 %[xa1], s61, %[xa1]\n\t" \
  "v_add_u32 %[xa2], s61, %[xa2]\n\t" \
  "v_add_u32 %[xa3], s61, %[xa3]\n\t" \
  "v_add_u32 %[wa0], s61, %[wa0]\n\t" \
  "v_mfma_f32_32x32x16_bf16 %[a0], v[116:119], v[96:99], %[a0]\n\t" \
  "v_mfma_f32_32x32x16_bf16 %[c0], v[116:119], v[156:159], %[c0]\n\t" \
  "ds_read_b128 v[96:99], %[xa0]\n\t" \
  "ds_read_b64_tr_b16 v[116:117], %[wa0] offset:0\n\t" \
  "ds_read_b64_tr_b16 v[118:119], %[wa0] offset:512\n\t" \
  "v_mfma_f32_32x32x16_bf16 %[a0], v[120:123], v[100:103], %[a0]\n\t" \
  "v_mfma_f32_32x32x16_bf16 %[c0], v[120:123], v[156:159], %[c0]\n\t" \
  "ds_read_b128 v[100:103], %[xa1]\n\t" \
  "ds_read_b64_tr_b16 v[120:121], %[wa0] offset:2048\n\t" \
  "ds_read_b64_tr_b16 v[122:123], %[wa0] offset:2560\n\t" \
  "v_mfma_f32_32x32x16_bf16 %[a0], v[124:127], v[104:107], %[a0]\n\t" \
  "ds_read_b128 v[104:107], %[xa2]\n\t" \
  "ds_read_b64_tr_b16 v[124:125], %[wa0] offset:4096\n\t" \
  "ds_read_b64_tr_b16 v[126:127], %[wa0] offset:4608\n\t" \
  "ds_read_b128 v[112:115], %[xa3]\n\t" \
  "ds_read_b64_tr_b16 v[148:149], %[wa0] offset:6144\n\t" \
  "ds_read_b64_tr_b16 v[150:151], %[wa0] offset:6656\n\t" \
  "v_mfma_f32_32x32x16_bf16 %[a0], v[128:131], v[108:111], %[a0]\n\t" \
  "s_waitcnt lgkmcnt(0)\n\t" \
  "v_mfma_f32_32x32x16_bf16 %[a0], v[116:119], v[96:99], %[a0]\n\t" \
  "v_mfma_f32_32x32x16_bf16 %[c0], v[116:119], v[156:159], %[c0]\n\t" \
  "v_mfma_f32_32x32x16_bf16 %[a0], v[120:123], v[100:103], %[a0]\n\t" \
  "v_mfma_f32_32x32x16_bf16 %[c0], v[120:123], v[156:159], %[c0]\n\t" \
  "v_mfma_f32_32x32x16_bf16 %[a0], v[124:127], v[104:107], %[a0]\n\t" \
  "v_mfma_f32_32x32x16_bf16 %[a0], v[148:151], v[112:115], %[a0]\n\t" \
  "s_nop 7\n\t" \
  "s_nop 7\n\t" \
  ""
#define P1_BODY_1_1 \
  "s_waitcnt lgkmcnt(0)\n\t" \
  "v_mov_b32 v156, 0x3f803f80\n\t" \
  "v_mov_b32 v157, 0x3f803f80\n\t" \
  "v_mov_b32 v158, 0x3f803f80\n\t" \
  "v_mov_b32 v159, 0x3f803f80\n\t" \
  "ds_read_b128 v[96:99], %[xa0]\n\t" \
  "ds_read_b64_tr_b16 v[116:117], %[wa0] offset:0\n\t" \
  "ds_read_b64_tr_b16 v[118:119], %[wa0] offset:512\n\t" \
  "ds_read_b128 v[100:103], %[xa1]\n\t" \
  "ds_read_b64_tr_b16 v[120:121], %[wa0] offset:2048\n\t" \
  "ds_read_b64_tr_b16 v[122:123], %[wa0] offset:2560\n\t" \
  "ds_read_b128 v[104:107], %[xa2]\n\t" \
  "ds_read_b64_tr_b16 v[124:125], %[wa0] offset:4096\n\t" \
  "ds_read_b64_tr_b16 v[126:127], %[wa0] offset:4608\n\t" \
  "ds_read_b128 v[108:111], %[xa3]\n\t" \
  "ds_read_b64_tr_b16 v[128:129], %[wa0] offset:6144\n\t" \
  "ds_read_b64_tr_b16 v[130:131], %[wa0] offset:6656\n\t" \
  "s_mov_b32 s60, 0\n\t" \
  "s_mov_b32 s62, 3\n\t" \
  "s_mov_b32 s63, 0xfffe8000\n\t" \
  "LOOP%=:\n\t" \
  "s_waitcnt lgkmcnt(0)\n\t" \
  "s_barrier\n\t" \
  "s_cmp_eq_u32 s60, 3\n\t" \
  "s_cselect_b32 s61, s63, 0x8000\n\t" \
  "s_add_u32 s60, s60, 1\n\t" \
  "s_and_b32 s60, s60, 3\n\t" \
  "v_add_u32 %[xa0], s61, %[xa0]\n\t" \
  "v_add_u32 %[xa1], s61, %[xa1]\n\t" \
  "v_add_u32 %[xa2], s61, %[xa2]\n\t" \
  "v_add_u32 %[xa3], s61, %[xa3]\n\t" \
  "v_add_u32 %[wa0], s61, %[wa0]\n\t" \
  "v_mfma_f32_32x32x16_bf16 %[a0], v[116:119], v[96:99], %[a0]\n\t" \
  "ds_read_b128 v[96:99], %[xa0]\n\t" \
  "ds_read_b64_tr_b16 v[116:117], %[wa0] offset:0\n\t" \
  "ds_read_b64_tr_b16 v[118:119], %[wa0] offset:512\n\t" \
  "v_mfma_f32_32x32x16_bf16 %[a0], v[120:123], v[100:103], %[a0]\n\t" \
  "ds_read_b128 v[100:103], %[xa1]\n\t" \
  "ds_read_b64_tr_b16 v[120:121], %[wa0] offset:2048\n\t" \
  "ds_read_b64_tr_b16 v[122:123], %[wa0] offset:2560\n\t" \
  "v_mfma_f32_32x32x16_bf16 %[a0], v[124:127], v[104:107], %[a0]\n\t" \
  "v_mfma_f32_32x32x16_bf16 %[c0], v[124:127], v[156:159], %[c0]\n\t" \
  "ds_read_b128 v[104:107], %[xa2]\n\t" \
  "ds_read_b64_tr_b16 v[124:125], %[wa0] offset:4096\n\t" \
  "ds_read_b64_tr_b16 v[126:127], %[wa0] offset:4608\n\t" \
  "ds_read_b128 v[112:115], %[xa3]\n\t" \
  "ds_read_b64_tr_b16 v[148:149], %[wa0] offset:6144\n\t" \
  "ds_read_b64_tr_b16 v[150:151], %[wa0] offset:6656\n\t" \
  "v_mfma_f32_32x32x16_bf16 %[a0], v[128:131], v[108:111], %[a0]\n\t" \
  "v_mfma_f32_32x32x16_bf16 %[c0], v[128:131], v[156:159], %[c0]\n\t" \
  "s_waitcnt lgkmcnt(0)\n\t" \
  "s_barrier\n\t" \
  "s_cmp_eq_u32 s60, 3\n\t" \
  "s_cselect_b32 s61, s63, 0x8000\n\t" \
  "s_add_u32 s60, s60, 1\n\t" \
  "s_and_b32 s60, s60, 3\n\t" \
  "v_add_u32 %[xa0], s61, %[xa0]\n\t" \
  "v_add_u32 %[xa1], s61, %[xa1]\n\t" \
  "v_add_u32 %[xa2], s61, %[xa2]\n\t" \
  "v_add_u32 %[xa3], s61, %[xa3]\n\t" \
  "v_add_u32 %[wa0], s61, %[wa0]\n\t" \
  "v_mfma_f32_32x32x16_bf16 %[a0], v[116:119], v[96:99], %[a0]\n\t" \
  "ds_read_b128 v[96:99], %[xa0]\n\t" \
  "ds_read_b64_tr_b16 v[116:117], %[wa0] offset:0\n\t" \
  "ds_read_b64_tr_b16 v[118:119], %[wa0] offset:512\n\t" \
  "v_mfma_f32_32x32x16_bf16 %[a0], v[120:123], v[100:103], %[a0]\n\t" \
  "ds_read_b128 v[100:103], %[xa1]\n\t" \
  "ds_read_b64_tr_b16 v[120:121], %[wa0] offset:2048\n\t" \
  "ds_read_b64_tr_b16 v[122:123], %[wa0] offset:2560\n\t" \
  "v_mfma_f32_32x32x16_bf16 %[a0], v[124:127], v[104:107], %[a0]\n\t" \
  "v_mfma_f32_32x32x16_bf16 %[c0], v[124:127], v[156:159], %[c0]\n\t" \
  "ds_read_b128 v[104:107], %[xa2]\n\t" \
  "ds_read_b64_tr_b16 v[124:125], %[wa0] offset:4096\n\t" \
  "ds_read_b64_tr_b16 v[126:127], %[wa0] offset:4608\n\t" \
  "ds_read_b128 v[108:111], %[xa3]\n\t" \
  "ds_read_b64_tr_b16 v[128:129], %[wa0] offset:6144\n\t" \
  "ds_read_b64_tr_b16 v[130:131], %[wa0] offset:6656\n\t" \
  "v_mfma_f32_32x32x16_bf16 %[a0], v[148:151], v[112:115], %[a0]\n\t" \
  "v_mfma_f32_32x32x16_bf16 %[c0], v[148:151], v[156:159], %[c0]\n\t" \
  "s_cmp_eq_u32 s62, 2\n\t" \
  "s_cbranch_scc0 NF%=\n\t" \
  "s_waitcnt vmcnt(0)\n\t" \
  "s_mov_b64 exec, 1\n\t" \
  "global_store_dwordx2 %[fxo], %[tag], %[ws] sc1\n\t" \
  "s_mov_b64 exec, -1\n\t" \
  "NF%=:\n\t" \
  "s_sub_u32 s62, s62, 1\n\t" \
  "s_cmp_lg_u32 s62, 0\n\t" \
  "s_cbranch_scc1 LOOP%=\n\t" \
  "s_waitcnt lgkmcnt(0)\n\t" \
  "s_barrier\n\t" \
  "s_cmp_eq_u32 s60, 3\n\t" \
  "s_cselect_b32 s61, s63, 0x8000\n\t" \
  "s_add_u32 s60, s60, 1\n\t" \
  "s_and_b32 s60, s60, 3\n\t" \
  "v_add_u32 %[xa0], s61, %[xa0]\n\t" \
  "v_add_u32 %[xa1], s61, %[xa1]\n\t" \
  "v_add_u32 %[xa2], s61, %[xa2]\n\t" \
  "v_add_u32 %[xa3], s61, %[xa3]\n\t" \
  "v_add_u32 %[wa0], s61, %[wa0]\n\t" \
  "v_mfma_f32_32x32x16_bf16 %[a0], v[116:119], v[96:99], %[a0]\n\t" \
  "ds_read_b128 v[96:99], %[xa0]\n\t" \
  "ds_read_b64_tr_b16 v[116:117], %[wa0] offset:0\n\t" \
  "ds_read_b64_tr_b16 v[118:119], %[wa0] offset:512\n\t" \
  "v_mfma_f32_32x32x16_bf16 %[a0], v[120:123], v[100:103], %[a0]\n\t" \
  "ds_read_b128 v[100:103], %[xa1]\n\t" \
  "ds_read_b64_tr_b16 v[120:121], %[wa0] offset:2048\n\t" \
  "ds_read_b64_tr_b16 v[122:123], %[wa0] offset:2560\n\t" \
  "v_mfma_f32_32x32x16_bf16 %[a0], v[124:127], v[104:107], %[a0]\n\t" \
  "v_mfma_f32_32x32x16_bf16 %[c0], v[124:127], v[156:159], %[c0]\n\t" \
  "ds_read_b128 v[104:107], %[xa2]\n\t" \
  "ds_read_b64_tr_b16 v[124:125], %[wa0] offset:4096\n\t" \
  "ds_read_b64_tr_b16 v[126:127], %[wa0] offset:4608\n\t" \
  "ds_read_b128 v[112:115], %[xa3]\n\t" \
  "ds_read_b64_tr_b16 v[148:149], %[wa0] offset:6144\n\t" \
  "ds_read_b64_tr_b16 v[150:151], %[wa0] offset:6656\n\t" \
  "v_mfma_f32_32x32x16_bf16 %[a0], v[128:131], v[108:111], %[a0]\n\t" \
  "v_mfma_f32_32x32x16_bf16 %[c0], v[128:131], v[156:159], %[c0]\n\t" \
  "s_waitcnt lgkmcnt(0)\n\t" \
  "v_mfma_f32_32x32x16_bf16 %[a0], v[116:119], v[96:99], %[a0]\n\t" \
  "v_mfma_f32_32x32x16_bf16 %[a0], v[120:123], v[100:103], %[a0]\n\t" \
  "v_mfma_f32_32x32x16_bf16 %[a0], v[124:127], v[104:107], %[a0]\n\t" \
  "v_mfma_f32_32x32x16_bf16 %[c0], v[124:127], v[156:159], %[c0]\n\t" \
  "v_mfma_f32_32x32x16_bf16 %[a0], v[148:151], v[112:115], %[a0]\n\t" \
  "v_mfma_f32_32x32x16_bf16 %[c0], v[148:151], v[156:159], %[c0]\n\t" \
  "s_nop 7\n\t" \
  "s_nop 7\n\t" \
  ""
#define P1_BODY_2_0 \
  "s_waitcnt lgkmcnt(0)\n\t" \
  "v_mov_b32 v156, 0x3f803f80\n\t" \
  "v_mov_b32 v157, 0x3f803f80\n\t" \
  "v_mov_b32 v158, 0x3f803f80\n\t" \
  "v_mov_b32 v159, 0x3f803f80\n\t" \
  "ds_read_b128 v[96:99], %[xa0]\n\t" \
  "ds_read_b64_tr_b16 v[116:117], %[wa0] offset:0\n\t" \
  "ds_read_b64_tr_b16 v[118:119], %[wa0] offset:512\n\t" \
  "ds_read_b64_tr_b16 v[120:121], %[wa1] offset:0\n\t" \
  "ds_read_b64_tr_b16 v[122:123], %[wa1] offset:512\n\t" \
  "ds_read_b128 v[100:103], %[xa1]\n\t" \
  "ds_read_b64_tr_b16 v[124:125], %[wa0] offset:2048\n\t" \
  "ds_read_b64_tr_b16 v[126:127], %[wa0] offset:2560\n\t" \
  "ds_read_b64_tr_b16 v[128:129], %[wa1] offset:2048\n\t" \
  "ds_read_b64_tr_b16 v[130:131], %[wa1] offset:2560\n\t" \
  "ds_read_b128 v[104:107], %[xa2]\n\t" \
  "ds_read_b64_tr_b16 v[132:133], %[wa0] offset:4096\n\t" \
  "ds_read_b64_tr_b16 v[134:135], %[wa0] offset:4608\n\t" \
  "ds_read_b64_tr_b16 v[136:137], %[wa1] offset:4096\n\t" \
  "ds_read_b64_tr_b16 v[138:139], %[wa1] offset:4608\n\t" \
  "ds_read_b128 v[108:111], %[xa3]\n\t" \
  "ds_read_b64_tr_b16 v[140:141], %[wa0] offset:6144\n\t" \
  "ds_read_b64_tr_b16 v[142:143], %[wa0] offset:6656\n\t" \
  "ds_read_b64_tr_b16 v[144:145], %[wa1] offset:6144\n\t" \
  "ds_read_b64_tr_b16 v[146:147], %[wa1] offset:6656\n\t" \
  "s_mov_b32 s60, 0\n\t" \
  "s_mov_b32 s62, 3\n\t" \
  "s_mov_b32 s63, 0xfffe8000\n\t" \
  "LOOP%=:\n\t" \
  "s_waitcnt lgkmcnt(0)\n\t" \
  "s_barrier\n\t" \
  "s_cmp_eq_u32 s60, 3\n\t" \
  "s_cselect_b32 s61, s63, 0x8000\n\t" \
  "s_add_u32 s60, s60, 1\n\t" \
  "s_and_b32 s60, s60, 3\n\t" \
  "v_add_u32 %[xa0], s61, %[xa0]\n\t" \
  "v_add_u32 %[xa1], s61, %[xa1]\n\t" \
  "v_add_u32 %[xa2], s61, %[xa2]\n\t" \
  "v_add_u32 %[xa3], s61, %[xa3]\n\t" \
  "v_add_u32 %[wa0], s61, %[wa0]\n\t" \
  "v_add_u32 %[wa1], s61, %[wa1]\n\t" \
  "v_mfma_f32_32x32x16_bf16 %[a0], v[116:119], v[96:99], %[a0]\n\t" \
  "v_mfma_f32_32x32x16_bf16 %[a1], v[96:99], v[120:123], %[a1]\n\t" \
  "v_mfma_f32_32x32x16_bf16 %[c0], v[116:119], v[156:159], %[c0]\n\t" \
  "v_mfma_f32_32x32x16_bf16 %[c1], v[156:159], v[120:123], %[c1]\n\t" \
  "ds_read_b128 v[96:99], %[xa0]\n\t" \
  "ds_read_b64_tr_b16 v[116:117], %[wa0] offset:0\n\t" \
  "ds_read_b64_tr_b16 v[118:119], %[wa0] offset:512\n\t" \
  "ds_read_b64_tr_b16 v[120:121], %[wa1] offset:0\n\t" \
  "ds_read_b64_tr_b16 v[122:123], %[wa1] offset:512\n\t" \
  "v_mfma_f32_32x32x16_bf16 %[a0], v[124:127], v[100:103], %[a0]\n\t" \
  "v_mfma_f32_32x32x16_bf16 %[a1], v[100:103], v[128:131], %[a1]\n\t" \
  "v_mfma_f32_32x32x16_bf16 %[c0], v[124:127], v[156:159], %[c0]\n\t" \
  "v_mfma_f32_32x32x16_bf16 %[c1], v[156:159], v[128:131], %[c1]\n\t" \
  "ds_read_b128 v[100:103], %[xa1]\n\t" \
  "ds_read_b64_tr_b16 v[124:125], %[wa0] offset:2048\n\t" \
  "ds_read_b64_tr_b16 v[126:127], %[wa0] offset:2560\n\t" \
  "ds_read_b64_tr_b16 v[128:129], %[wa1] offset:2048\n\t" \
  "ds_read_b64_tr_b16 v[130:131], %[wa1] offset:2560\n\t" \
  "v_mfma_f32_32x32x16_bf16 %[a0], v[132:135], v[104:107], %[a0]\n\t" \
  "v_mfma_f32_32x32x16_bf16 %[a1], v[104:107], v[136:139], %[a1]\n\t" \
  "ds_read_b128 v[104:107], %[xa2]\n\t" \
  "ds_read_b64_tr_b16 v[132:133], %[wa0] offset:4096\n\t" \
  "ds_read_b64_tr_b16 v[134:135], %[wa0] offset:4608\n\t" \
  "ds_read_b64_tr_b16 v[136:137], %[wa1] offset:4096\n\t" \
  "ds_read_b64_tr_b16 v[138:139], %[wa1] offset:4608\n\t" \
  "ds_read_b128 v[112:115], %[xa3]\n\t" \
  "ds_read_b64_tr_b16 v[148:149], %[wa0] offset:6144\n\t" \
  "ds_read_b64_tr_b16 v[150:151], %[wa0] offset:6656\n\t" \
  "ds_read_b64_tr_b16 v[152:153], %[wa1] offset:6144\n\t" \
  "ds_read_b64_tr_b16 v[154:155], %[wa1] offset:6656\n\t" \
  "v_mfma_f32_32x32x16_bf16 %[a0], v[140:143], v[108:111], %[a0]\n\t" \
  "v_mfma_f32_32x32x16_bf16 %[a1], v[108:111], v[144:147], %[a1]\n\t" \
  "s_waitcnt lgkmcnt(0)\n\t" \
  "s_barrier\n\t" \
  "s_cmp_eq_u32 s60, 3\n\t" \
  "s_cselect_b32 s61, s63, 0x8000\n\t" \
  "s_add_u32 s60, s60, 1\n\t" \
  "s_and_b32 s60, s60, 3\n\t" \
  "v_add_u32 %[xa0], s61, %[xa0]\n\t" \
  "v_add_u32 %[xa1], s61, %[xa1]\n\t" \
  "v_add_u32 %[xa2], s61, %[xa2]\n\t" \
  "v_add_u32 %[xa3], s61, %[xa3]\n\t" \
  "v_add_u32 %[wa0], s61, %[wa0]\n\t" \
  "v_add_u32 %[wa1], s61, %[wa1]\n\t" \
  "v_mfma_f32_32x32x16_bf16 %[a0], v[116:119], v[96:99], %[a0]\n\t" \
  "v_mfma_f32_32x32x16_bf16 %[a1], v[96:99], v[120:123], %[a1]\n\t" \
  "v_mfma_f32_32x32x16_bf16 %[c0], v[116:119], v[156:159], %[c0]\n\t" \
  "v_mfma_f32_32x32x16_bf16 %[c1], v[156:159], v[120:123], %[c1]\n\t" \
  "ds_read_b128 v[96:99], %[xa0]\n\t" \
  "ds_read_b64_tr_b16 v[116:117], %[wa0] offset:0\n\t" \
  "ds_read_b64_tr_b16 v[118:119], %[wa0] offset:512\n\t" \
  "ds_read_b64_tr_b16 v[120:121], %[wa1] offset:0\n\t" \
  "ds_read_b64_tr_b16 v[122:123], %[wa1] offset:512\n\t" \
  "v_mfma_f32_32x32x16_bf16 %[a0], v[124:127], v[100:103], %[a0]\n\t" \
  "v_mfma_f32_32x32x16_bf16 %[a1], v[100:103], v[128:131], %[a1]\n\t" \
  "v_mfma_f32_32x32x16_bf16 %[c0], v[124:127], v[156:159], %[c0]\n\t" \
  "v_mfma_f32_32x32x16_bf16 %[c1], v[156:159], v[128:131], %[c1]\n\t" \
  "ds_read_b128 v[100:103], %[xa1]\n\t" \
  "ds_read_b64_tr_b16 v[124:125], %[wa0] offset:2048\n\t" \
  "ds_read_b64_tr_b16 v[126:127], %[wa0] offset:2560\n\t" \
  "ds_read_b64_tr_b16 v[128:129], %[wa1] offset:2048\n\t" \
  "ds_read_b64_tr_b16 v[130:131], %[wa1] offset:2560\n\t" \
  "v_mfma_f32_32x32x16_bf16 %[a0], v[132:135], v[104:107], %[a0]\n\t" \
  "v_mfma_f32_32x32x16_bf16 %[a1], v[104:107], v[136:139], %[a1]\n\t" \
  "ds_read_b128 v[104:107], %[xa2]\n\t" \
  "ds_read_b64_tr_b16 v[132:133], %[wa0] offset:4096\n\t" \
  "ds_read_b64_tr_b16 v[134:135], %[wa0] offset:4608\n\t" \
  "ds_read_b64_tr_b16 v[136:137], %[wa1] offset:4096\n\t" \
  "ds_read_b64_tr_b16 v[138:139], %[wa1] offset:4608\n\t" \
  "ds_read_b128 v[108:111], %[xa3]\n\t" \
  "ds_read_b64_tr_b16 v[140:141], %[wa0] offset:6144\n\t" \
  "ds_read_b64_tr_b16 v[142:143], %[wa0] offset:6656\n\t" \
  "ds_read_b64_tr_b16 v[144:145], %[wa1] offset:6144\n\t" \
  "ds_read_b64_tr_b16 v[146:147], %[wa1] offset:6656\n\t" \
  "v_mfma_f32_32x32x16_bf16 %[a0], v[148:151], v[112:115], %[a0]\n\t" \
  "v_mfma_f32_32x32x16_bf16 %[a1], v[112:115], v[152:155], %[a1]\n\t" \
  "s_cmp_eq_u32 s62, 2\n\t" \
  "s_cbranch_scc0 NF%=\n\t" \
  "s_waitcnt vmcnt(0)\n\t" \
  "s_mov_b64 exec, 1\n\t" \
  "global_store_dwordx2 %[fxo], %[tag], %[ws] sc1\n\t" \
  "s_mov_b64 exec, -1\n\t" \
  "NF%=:\n\t" \
  "s_sub_u32 s62, s62, 1\n\t" \
  "s_cmp_lg_u32 s62, 0\n\t" \
  "s_cbranch_scc1 LOOP%=\n\t" \
  "s_waitcnt lgkmcnt(0)\n\t" \
  "s_barrier\n\t" \
  "s_cmp_eq_u32 s60, 3\n\t" \
  "s_cselect_b32 s61, s63, 0x8000\n\t" \
  "s_add_u32 s60, s60, 1\n\t" \
  "s_and_b32 s60, s60, 3\n\t" \
  "v_add_u32 %[xa0], s61, %[xa0]\n\t" \
  "v_add_u32 %[xa1], s61, %[xa1]\n\t" \
  "v_add_u32 %[xa2], s61, %[xa2]\n\t" \
  "v_add_u32 %[xa3], s61, %[xa3]\n\t" \
  "v_add_u32 %[wa0], s61, %[wa0]\n\t" \
  "v_add_u32 %[wa1], s61, %[wa1]\n\t" \
  "v_mfma_f32_32x32x16_bf16 %[a0], v[116:119], v[96:99], %[a0]\n\t" \
  "v_mfma_f32_32x32x16_bf16 %[a1], v[96:99], v[120:123], %[a1]\n\t" \
  "v_mfma_f32_32x32x16_bf16 %[c0], v[116:119], v[156:159], %[c0]\n\t" \
  "v_mfma_f32_32x32x16_bf16 %[c1], v[156:159], v[120:123], %[c1]\n\t" \
  "ds_read_b128 v[96:99], %[xa0]\n\t" \
  "ds_read_b64_tr_b16 v[116:117], %[wa0] offset:0\n\t" \
  "ds_read_b64_tr_b16 v[118:119], %[wa0] offset:512\n\t" \
  "ds_read_b64_tr_b16 v[120:121], %[wa1] offset:0\n\t" \
  "ds_read_b64_tr_b16 v[122:123], %[wa1] offset:512\n\t" \
  "v_mfma_f32_32x32x16_bf16 %[a0], v[124:127], v[100:103], %[a0]\n\t" \
  "v_mfma_f32_32x32x16_bf16 %[a1], v[100:103], v[128:131], %[a1]\n\t" \
  "v_mfma_f32_32x32x16_bf16 %[c0], v[124:127], v[156:159], %[c0]\n\t" \
  "v_mfma_f32_32x32x16_bf16 %[c1], v[156:159], v[128:131], %[c1]\n\t" \
  "ds_read_b128 v[100:103], %[xa1]\n\t" \
  "ds_read_b64_tr_b16 v[124:125], %[wa0] offset:2048\n\t" \
  "ds_read_b64_tr_b16 v[126:127], %[wa0] offset:2560\n\t" \
  "ds_read_b64_tr_b16 v[128:129], %[wa1] offset:2048\n\t" \
  "ds_read_b64_tr_b16 v[130:131], %[wa1] offset:2560\n\t" \
  "v_mfma_f32_32x32x16_bf16 %[a0], v[132:135], v[104:107], %[a0]\n\t" \
  "v_mfma_f32_32x32x16_bf16 %[a1], v[104:107], v[136:139], %[a1]\n\t" \
  "ds_read_b128 v[104:107], %[xa2]\n\t" \
  "ds_read_b64_tr_b16 v[132:133], %[wa0] offset:4096\n\t" \
  "ds_read_b64_tr_b16 v[134:135], %[wa0] offset:4608\n\t" \
  "ds_read_b64_tr_b16 v[136:137], %[wa1] offset:4096\n\t" \
  "ds_read_b64_tr_b16 v[138:139], %[wa1] offset:4608\n\t" \
  "ds_read_b128 v[112:115], %[xa3]\n\t" \
  "ds_read_b64_tr_b16 v[148:149], %[wa0] offset:6144\n\t" \
  "ds_read_b64_tr_b16 v[150:151], %[wa0] offset:6656\n\t" \
  "ds_read_b64_tr_b16 v[152:153], %[wa1] offset:6144\n\t" \
  "ds_read_b64_tr_b16 v[154:155], %[wa1] offset:6656\n\t" \
  "v_mfma_f32_32x32x16_bf16 %[a0], v[140:143], v[108:111], %[a0]\n\t" \
  "v_mfma_f32_32x32x16_bf16 %[a1], v[108:111], v[144:147], %[a1]\n\t" \
  "s_waitcnt lgkmcnt(0)\n\t" \
  "v_mfma_f32_32x32x16_bf16 %[a0], v[116:119], v[96:99], %[a0]\n\t" \
  "v_mfma_f32_32x32x16_bf16 %[a1], v[96:99], v[120:123], %[a1]\n\t" \
  "v_mfma_f32_32x32x16_bf16 %[c0], v[116:119], v[156:159], %[c0]\n\t" \
  "v_mfma_f32_32x32x16_bf16 %[c1], v[156:159], v[120:123], %[c1]\n\t" \
  "v_mfma_f32_32x32x16_bf16 %[a0], v[124:127], v[100:103], %[a0]\n\t" \
  "v_mfma_f32_32x32x16_bf16 %[a1], v[100:103], v[128:131], %[a1]\n\t" \
  "v_mfma_f32_32x32x16_bf16 %[c0], v[124:127], v[156:159], %[c0]\n\t" \
  "v_mfma_f32_32x32x16_bf16 %[c1], v[156:159], v[128:131], %[c1]\n\t" \
  "v_mfma_f32_32x32x16_bf16 %[a0], v[132:135], v[104:107], %[a0]\n\t" \
  "v_mfma_f32_32x32x16_bf16 %[a1], v[104:107], v[136:139], %[a1]\n\t" \
  "v_mfma_f32_32x32x16_bf16 %[a0], v[148:151], v[112:115], %[a0]\n\t" \
  "v_mfma_f32_32x32x16_bf16 %[a1], v[112:115], v[152:155], %[a1]\n\t" \
  "s_nop 7\n\t" \
  "s_nop 7\n\t" \
  ""
#define P1_BODY_2_1 \
  "s_waitcnt lgkmcnt(0)\n\t" \
  "v_mov_b32 v156, 0x3f803f80\n\t" \
  "v_mov_b32 v157, 0x3f803f80\n\t" \
  "v_mov_b32 v158, 0x3f803f80\n\t" \
  "v_mov_b32 v159, 0x3f803f80\n\t" \
  "ds_read_b128 v[96:99], %[xa0]\n\t" \
  "ds_read_b64_tr_b16 v[116:117], %[wa0] offset:0\n\t" \
  "ds_read_b64_tr_b16 v[118:119], %[wa0] offset:512\n\t" \
  "ds_read_b64_tr_b16 v[120:121], %[wa1] offset:0\n\t" \
  "ds_read_b64_tr_b16 v[122:123], %[wa1] offset:512\n\t" \
  "ds_read_b128 v[100:103], %[xa1]\n\t" \
  "ds_read_b64_tr_b16 v[124:125], %[wa0] offset:2048\n\t" \
  "ds_read_b64_tr_b16 v[126:127], %[wa0] offset:2560\n\t" \
  "ds_read_b64_tr_b16 v[128:129], %[wa1] offset:2048\n\t" \
  "ds_read_b64_tr_b16 v[130:131], %[wa1] offset:2560\n\t" \
  "ds_read_b128 v[104:107], %[xa2]\n\t" \
  "ds_read_b64_tr_b16 v[132:133], %[wa0] offset:4096\n\t" \
  "ds_read_b64_tr_b16 v[134:135], %[wa0] offset:4608\n\t" \
  "ds_read_b64_tr_b16 v[136:137], %[wa1] offset:4096\n\t" \
  "ds_read_b64_tr_b16 v[138:139], %[wa1] offset:4608\n\t" \
  "ds_read_b128 v[108:111], %[xa3]\n\t" \
  "ds_read_b64_tr_b16 v[140:141], %[wa0] offset:6144\n\t" \
  "ds_read_b64_tr_b16 v[142:143], %[wa0] offset:6656\n\t" \
  "ds_read_b64_tr_b16 v[144:145], %[wa1] offset:6144\n\t" \
  "ds_read_b64_tr_b16 v[146:147], %[wa1] offset:6656\n\t" \
  "s_mov_b32 s60, 0\n\t" \
  "s_mov_b32 s62, 3\n\t" \
  "s_mov_b32 s63, 0xfffe8000\n\t" \
  "LOOP%=:\n\t" \
  "s_waitcnt lgkmcnt(0)\n\t" \
  "s_barrier\n\t" \
  "s_cmp_eq_u32 s60, 3\n\t" \
  "s_cselect_b32 s61, s63, 0x8000\n\t" \
  "s_add_u32 s60, s60, 1\n\t" \
  "s_and_b32 s60, s60, 3\n\t" \
  "v_add_u32 %[xa0], s61, %[xa0]\n\t" \
  "v_add_u32 %[xa1], s61, %[xa1]\n\t" \
  "v_add_u32 %[xa2], s61, %[xa2]\n\t" \
  "v_add_u32 %[xa3], s61, %[xa3]\n\t" \
  "v_add_u32 %[wa0], s61, %[wa0]\n\t" \
  "v_add_u32 %[wa1], s61, %[wa1]\n\t" \
  "v_mfma_f32_32x32x16_bf16 %[a0], v[116:119], v[96:99], %[a0]\n\t" \
  "v_mfma_f32_32x32x16_bf16 %[a1], v[96:99], v[120:123], %[a1]\n\t" \
  "ds_read_b128 v[96:99], %[xa0]\n\t" \
  "ds_read_b64_tr_b16 v[116:117], %[wa0] offset:0\n\t" \
  "ds_read_b64_tr_b16 v[118:119], %[wa0] offset:512\n\t" \
  "ds_read_b64_tr_b16 v[120:121], %[wa1] offset:0\n\t" \
  "ds_read_b64_tr_b16 v[122:123], %[wa1] offset:512\n\t" \
  "v_mfma_f32_32x32x16_bf16 %[a0], v[124:127], v[100:103], %[a0]\n\t" \
  "v_mfma_f32_32x32x16_bf16 %[a1], v[100:103], v[128:131], %[a1]\n\t" \
  "ds_read_b128 v[100:103], %[xa1]\n\t" \
  "ds_read_b64_tr_b16 v[124:125], %[wa0] offset:2048\n\t" \
  "ds_read_b64_tr_b16 v[126:127], %[wa0] offset:2560\n\t" \
  "ds_read_b64_tr_b16 v[128:129], %[wa1] offset:2048\n\t" \
  "ds_read_b64_tr_b16 v[130:131], %[wa1] offset:2560\n\t" \
  "v_mfma_f32_32x32x16_bf16 %[a0], v[132:135], v[104:107], %[a0]\n\t" \
  "v_mfma_f32_32x32x16_bf16 %[a1], v[104:107], v[136:139], %[a1]\n\t" \
  "v_mfma_f32_32x32x16_bf16 %[c0], v[132:135], v[156:159], %[c0]\n\t" \
  "v_mfma_f32_32x32x16_bf16 %[c1], v[156:159], v[136:139], %[c1]\n\t" \
  "ds_read_b128 v[104:107], %[xa2]\n\t" \
  "ds_read_b64_tr_b16 v[132:133], %[wa0] offset:4096\n\t" \
  "ds_read_b64_tr_b16 v[134:135], %[wa0] offset:4608\n\t" \
  "ds_read_b64_tr_b16 v[136:137], %[wa1] offset:4096\n\t" \
  "ds_read_b64_tr_b16 v[138:139], %[wa1] offset:4608\n\t" \
  "ds_read_b128 v[112:115], %[xa3]\n\t" \
  "ds_read_b64_tr_b16 v[148:149], %[wa0] offset:6144\n\t" \
  "ds_read_b64_tr_b16 v[150:151], %[wa0] offset:6656\n\t" \
  "ds_read_b64_tr_b16 v[152:153], %[wa1] offset:6144\n\t" \
  "ds_read_b64_tr_b16 v[154:155], %[wa1] offset:6656\n\t" \
  "v_mfma_f32_32x32x16_bf16 %[a0], v[140:143], v[108:111], %[a0]\n\t" \
  "v_mfma_f32_32x32x16_bf16 %[a1], v[108:111], v[144:147], %[a1]\n\t" \
  "v_mfma_f32_32x32x16_bf16 %[c0], v[140:143], v[156:159], %[c0]\n\t" \
  "v_mfma_f32_32x32x16_bf16 %[c1], v[156:159], v[144:147], %[c1]\n\t" \
  "s_waitcnt lgkmcnt(0)\n\t" \
  "s_barrier\n\t" \
  "s_cmp_eq_u32 s60, 3\n\t" \
  "s_cselect_b32 s61, s63, 0x8000\n\t" \
  "s_add_u32 s60, s60, 1\n\t" \
  "s_and_b32 s60, s60, 3\n\t" \
  "v_add_u32 %[xa0], s61, %[xa0]\n\t" \
  "v_add_u32 %[xa1], s61, %[xa1]\n\t" \
  "v_add_u32 %[xa2], s61, %[xa2]\n\t" \
  "v_add_u32 %[xa3], s61, %[xa3]\n\t" \
  "v_add_u32 %[wa0], s61, %[wa0]\n\t" \
  "v_add_u32 %[wa1], s61, %[wa1]\n\t" \
  "v_mfma_f32_32x32x16_bf16 %[a0], v[116:119], v[96:99], %[a0]\n\t" \
  "v_mfma_f32_32x32x16_bf16 %[a1], v[96:99], v[120:123], %[a1]\n\t" \
  "ds_read_b128 v[96:99], %[xa0]\n\t" \
  "ds_read_b64_tr_b16 v[116:117], %[wa0] offset:0\n\t" \
  "ds_read_b64_tr_b16 v[118:119], %[wa0] offset:512\n\t" \
  "ds_read_b64_tr_b16 v[120:121], %[wa1] offset:0\n\t" \
  "ds_read_b64_tr_b16 v[122:123], %[wa1] offset:512\n\t" \
  "v_mfma_f32_32x32x16_bf16 %[a0], v[124:127], v[100:103], %[a0]\n\t" \
  "v_mfma_f32_32x32x16_bf16 %[a1], v[100:103], v[128:131], %[a1]\n\t" \
  "ds_read_b128 v[100:103], %[xa1]\n\t" \
  "ds_read_b64_tr_b16 v[124:125], %[wa0] offset:2048\n\t" \
  "ds_read_b64_tr_b16 v[126:127], %[wa0] offset:2560\n\t" \
  "ds_read_b64_tr_b16 v[128:129], %[wa1] offset:2048\n\t" \
  "ds_read_b64_tr_b16 v[130:131], %[wa1] offset:2560\n\t" \
  "v_mfma_f32_32x32x16_bf16 %[a0], v[132:135], v[104:107], %[a0]\n\t" \
  "v_mfma_f32_32x32x16_bf16 %[a1], v[104:107], v[136:139], %[a1]\n\t" \
  "v_mfma_f32_32x32x16_bf16 %[c0], v[132:135], v[156:159], %[c0]\n\t" \
  "v_mfma_f32_32x32x16_bf16 %[c1], v[156:159], v[136:139], %[c1]\n\t" \
  "ds_read_b128 v[104:107], %[xa2]\n\t" \
  "ds_read_b64_tr_b16 v[132:133], %[wa0] offset:4096\n\t" \
  "ds_read_b64_tr_b16 v[134:135], %[wa0] offset:4608\n\t" \
  "ds_read_b64_tr_b16 v[136:137], %[wa1] offset:4096\n\t" \
  "ds_read_b64_tr_b16 v[138:139], %[wa1] offset:4608\n\t" \
  "ds_read_b128 v[108:111], %[xa3]\n\t" \
  "ds_read_b64_tr_b16 v[140:141], %[wa0] offset:6144\n\t" \
  "ds_read_b64_tr_b16 v[142:143], %[wa0] offset:6656\n\t" \
  "ds_read_b64_tr_b16 v[144:145], %[wa1] offset:6144\n\t" \
  "ds_read_b64_tr_b16 v[146:147], %[wa1] offset:6656\n\t" \
  "v_mfma_f32_32x32x16_bf16 %[a0], v[148:151], v[112:115], %[a0]\n\t" \
  "v_mfma_f32_32x32x16_bf16 %[a1], v[112:115], v[152:155], %[a1]\n\t" \
  "v_mfma_f32_32x32x16_bf16 %[c0], v[148:151], v[156:159], %[c0]\n\t" \
  "v_mfma_f32_32x32x16_bf16 %[c1], v[156:159], v[152:155], %[c1]\n\t" \
  "s_cmp_eq_u32 s62, 2\n\t" \
  "s_cbranch_scc0 NF%=\n\t" \
  "s_waitcnt vmcnt(0)\n\t" \
  "s_mov_b64 exec, 1\n\t" \
  "global_store_dwordx2 %[fxo], %[tag], %[ws] sc1\n\t" \
  "s_mov_b64 exec, -1\n\t" \
  "NF%=:\n\t" \
  "s_sub_u32 s62, s62, 1\n\t" \
  "s_cmp_lg_u32 s62, 0\n\t" \
  "s_cbranch_scc1 LOOP%=\n\t" \
  "s_waitcnt lgkmcnt(0)\n\t" \
  "s_barrier\n\t" \
  "s_cmp_eq_u32 s60, 3\n\t" \
  "s_cselect_b32 s61, s63, 0x8000\n\t" \
  "s_add_u32 s60, s60, 1\n\t" \
  "s_and_b32 s60, s60, 3\n\t" \
  "v_add_u32 %[xa0], s61, %[xa0]\n\t" \
  "v_add_u32 %[xa1], s61, %[xa1]\n\t" \
  "v_add_u32 %[xa2], s61, %[xa2]\n\t" \
  "v_add_u32 %[xa3], s61, %[xa3]\n\t" \
  "v_add_u32 %[wa0], s61, %[wa0]\n\t" \
  "v_add_u32 %[wa1], s61, %[wa1]\n\t" \
  "v_mfma_f32_32x32x16_bf16 %[a0], v[116:119], v[96:99], %[a0]\n\t" \
  "v_mfma_f32_32x32x16_bf16 %[a1], v[96:99], v[120:123], %[a1]\n\t" \
  "ds_read_b128 v[96:99], %[xa0]\n\t" \
  "ds_read_b64_tr_b16 v[116:117], %[wa0] offset:0\n\t" \
  "ds_read_b64_tr_b16 v[118:119], %[wa0] offset:512\n\t" \
  "ds_read_b64_tr_b16 v[120:121], %[wa1] offset:0\n\t" \
  "ds_read_b64_tr_b16 v[122:123], %[wa1] offset:512\n\t" \
  "v_mfma_f32_32x32x16_bf16 %[a0], v[124:127], v[100:103], %[a0]\n\t" \
  "v_mfma_f32_32x32x16_bf16 %[a1], v[100:103], v[128:131], %[a1]\n\t" \
  "ds_read_b128 v[100:103], %[xa1]\n\t" \
  "ds_read_b64_tr_b16 v[124:125], %[wa0] offset:2048\n\t" \
  "ds_read_b64_tr_b16 v[126:127], %[wa0] offset:2560\n\t" \
  "ds_read_b64_tr_b16 v[128:129], %[wa1] offset:2048\n\t" \
  "ds_read_b64_tr_b16 v[130:131], %[wa1] offset:2560\n\t" \
  "v_mfma_f32_32x32x16_bf16 %[a0], v[132:135], v[104:107], %[a0]\n\t" \
  "v_mfma_f32_32x32x16_bf16 %[a1], v[104:107], v[136:139], %[a1]\n\t" \
  "v_mfma_f32_32x32x16_bf16 %[c0], v[132:135], v[156:159], %[c0]\n\t" \
  "v_mfma_f32_32x32x16_bf16 %[c1], v[156:159], v[136:139], %[c1]\n\t" \
  "ds_read_b128 v[104:107], %[xa2]\n\t" \
  "ds_read_b64_tr_b16 v[132:133], %[wa0] offset:4096\n\t" \
  "ds_read_b64_tr_b16 v[134:135], %[wa0] offset:4608\n\t" \
  "ds_read_b64_tr_b16 v[136:137], %[wa1] offset:4096\n\t" \
  "ds_read_b64_tr_b16 v[138:139], %[wa1] offset:4608\n\t" \
  "ds_read_b128 v[112:115], %[xa3]\n\t" \
  "ds_read_b64_tr_b16 v[148:149], %[wa0] offset:6144\n\t" \
  "ds_read_b64_tr_b16 v[150:151], %[wa0] offset:6656\n\t" \
  "ds_read_b64_tr_b16 v[152:153], %[wa1] offset:6144\n\t" \
  "ds_read_b64_tr_b16 v[154:155], %[wa1] offset:6656\n\t" \
  "v_mfma_f32_32x32x16_bf16 %[a0], v[140:143], v[108:111], %[a0]\n\t" \
  "v_mfma_f32_32x32x16_bf16 %[a1], v[108:111], v[144:147], %[a1]\n\t" \
  "v_mfma_f32_32x32x16_bf16 %[c0], v[140:143], v[156:159], %[c0]\n\t" \
  "v_mfma_f32_32x32x16_bf16 %[c1], v[156:159], v[144:147], %[c1]\n\t" \
  "s_waitcnt lgkmcnt(0)\n\t" \
  "v_mfma_f32_32x32x16_bf16 %[a0], v[116:119], v[96:99], %[a0]\n\t" \
  "v_mfma_f32_32x32x16_bf16 %[a1], v[96:99], v[120:123], %[a1]\n\t" \
  "v_mfma_f32_32x32x16_bf16 %[a0], v[124:127], v[100:103], %[a0]\n\t" \
  "v_mfma_f32_32x32x16_bf16 %[a1], v[100:103], v[128:131], %[a1]\n\t" \
  "v_mfma_f32_32x32x16_bf16 %[a0], v[132:135], v[104:107], %[a0]\n\t" \
  "v_mfma_f32_32x32x16_bf16 %[a1], v[104:107], v[136:139], %[a1]\n\t" \
  "v_mfma_f32_32x32x16_bf16 %[c0], v[132:135], v[156:159], %[c0]\n\t" \
  "v_mfma_f32_32x32x16_bf16 %[c1], v[156:159], v[136:139], %[c1]\n\t" \
  "v_mfma_f32_32x32x16_bf16 %[a0], v[148:151], v[112:115], %[a0]\n\t" \
  "v_mfma_f32_32x32x16_bf16 %[a1], v[112:115], v[152:155], %[a1]\n\t" \
  "v_mfma_f32_32x32x16_bf16 %[c0], v[148:151], v[156:159], %[c0]\n\t" \
  "v_mfma_f32_32x32x16_bf16 %[c1], v[156:159], v[152:155], %[c1]\n\t" \
  "s_nop 7\n\t" \
  "s_nop 7\n\t" \
  ""
#define P1_CLOB "v96","v97","v98","v99","v100","v101","v102","v103","v104","v105","v106","v107","v108","v109","v110","v111","v112","v113","v114","v115","v116","v117","v118","v119","v120","v121","v122","v123","v124","v125","v126","v127","v128","v129","v130","v131","v132","v133","v134","v135","v136","v137","v138","v139","v140","v141","v142","v143","v144","v145","v146","v147","v148","v149","v150","v151","v152","v153","v154","v155","v156","v157","v158","v159","s60","s61","s62","s63"

DI void l2_touch(const KP& p, char* lds, int w, int lane) {
    const int c = blockIdx.x & 7, t = blockIdx.x >> 3;
    const int jf = 4 * (c >> 1) + (t >> 3), P0 = jf < 11 ? 3 * jf : 33 + 2 * (jf - 11), np = jf < 11 ? 3 : 2;
    const unsigned k = 64 * (t & 7) + (((w & 3) * 64 + lane) >> 2); unsigned pp = lane & 3; pp = (int)pp < np ? pp : 0;
    asm volatile("s_mov_b32 m0, %2\n\ts_nop 0\n\tglobal_load_lds_dword %0, %1 sc1" :: "v"(k * (FF * 2) + (P0 + pp) * 64), "s"(w < 4 ? p.w1 : p.w3), "s"((unsigned)(uintptr_t)lds + L_CNT + 256) : "memory");
}

template <int CH>
DI void p1_a(const KP& p, char* lds, int i, int j, int w, int lane, u64 tag, const u32x4 xr, int lb, u32x4& jobf TSARG) {
    const int RT = w & 1, cg = (w >> 1) & 1;
    const int r = lane & 31, hh = lane >> 5, gp = (lane >> 4) & 1;
    const unsigned lds0 = (unsigned)(uintptr_t)lds;
    const __amdgpu_buffer_rsrc_t wsr = mkrsrc(p.ws, 10u << 20);
    float mrow, rsrow;
    { float s1 = 0.f, s2 = 0.f;
      for (int q = 0; q < 4; ++q) { const unsigned xq = xr[q]; s1 = dot2(xq, 0x3F803F80u, s1); s2 = dot2(xq, xq, s2); }
      s1 = wave_sum(s1); s2 = wave_sum(s2);
      const float m = s1 * (1.0f / DM); float var = s2 * (1.0f / DM) - m * m; var = var > 0.f ? var : 0.f;
      mrow = rbf(m); rsrow = __builtin_amdgcn_rcpf(rbf(__builtin_amdgcn_sqrtf(rbf(rbf(var) + 1e-5f))));
      u32x4 o;
      for (int q = 0; q < 4; ++q) { const unsigned d = cvtpk(__uint_as_float(xr[q] << 16) - mrow, __uint_as_float(xr[q] & 0xFFFF0000u) - mrow); o[q] = cvtpk(__uint_as_float(d << 16) * rsrow, __uint_as_float(d & 0xFFFF0000u) * rsrow); }
      *(LAS u32x4*)(lds + L_RING + 8192 + w * 1024 + ((lane ^ w) * 16)) = o; }
    if (lane == 0) { const u32x4 rec = {__float_as_uint(mrow), (unsigned)tag, __float_as_uint(rsrow), (unsigned)tag};
        st_sc1(mkrsrc(p.ws + OFF_ST, 32768), (lb * 8 + w) * 16, rec); }
    unsigned xa0, xa1, xa2, xa3;
    { const unsigned xb = lds0 + (32 * RT + r) * 128, sw = (r >> 1) & 7;
      xa0 = xb + 16 * ((0 + hh) ^ sw); xa1 = xb + 16 * ((2 + hh) ^ sw); xa2 = xb + 16 * ((4 + hh) ^ sw); xa3 = xb + 16 * ((6 + hh) ^ sw); }
    unsigned wa0, wa1;
    { const int q = (lane & 15) >> 2, sw = ((q >> 1) & 1) << 1, rowb = (8 * hh + q) * 128 + 8 * (lane & 3), hd = 2 * cg + CH;
      wa0 = lds0 + P1_XB + gp * 8192 + rowb + 32 * (hd ^ sw ^ gp);
      wa1 = lds0 + P1_XB + 16384 + rowb + 32 * ((2 * cg + gp) ^ sw); }
    WAITL(); BAR();
    STAMPK(1);
    {
      const int pc = 8 * w + (lane >> 3), row = lane & 7;
      const u32x4 o = *(const LAS u32x4*)(lds + L_RING + 8192 + row * 1024 + ((pc ^ row) * 16));
      st_sc1(wsr, OFF_XF + (lb >> 2) * 32768 + (pc >> 1) * 1024 + (8 * (lb & 3) + row + 32 * (pc & 1)) * 16, o); }
    f32x16 a0 = {}, a1 = {}, c0 = {}, c1 = {};
    const unsigned fxo = OFF_FX + (lb * 8 + w) * 64;
#define P1_RUN2(B) asm volatile(B : [a0] "+v"(a0), [a1] "+v"(a1), [c0] "+v"(c0), [c1] "+v"(c1), [xa0] "+v"(xa0), [xa1] "+v"(xa1), [xa2] "+v"(xa2), [xa3] "+v"(xa3), [wa0] "+v"(wa0), [wa1] "+v"(wa1) \
        : [fxo] "v"(fxo), [tag] "v"(tag), [ws] "s"(p.ws) : P1_CLOB, "scc", "memory")
#define P1_RUN1(B) asm volatile(B : [a0] "+v"(a0), [c0] "+v"(c0), [xa0] "+v"(xa0), [xa1] "+v"(xa1), [xa2] "+v"(xa2), [xa3] "+v"(xa3), [wa0] "+v"(wa0) \
        : [fxo] "v"(fxo), [tag] "v"(tag), [ws] "s"(p.ws) : P1_CLOB, "scc", "memory")
    if constexpr (CH == 0) { if (RT == 0) P1_RUN2(P1_BODY_2_0); else P1_RUN2(P1_BODY_2_1); } else { if (RT == 0) P1_RUN1(P1_BODY_1_0); else P1_RUN1(P1_BODY_1_1); }
    STAMPK(2);
    LAS float* CX = (LAS float*)(lds + L_RING + 16384);
    if ((lane & 31) == 0) {
#pragma unroll
        for (int g = 0; g < 4; ++g) *(LAS f32x4*)(CX + w * 96 + 8 * g + 4 * hh) = f32x4{c0[4 * g], c0[4 * g + 1], c0[4 * g + 2], c0[4 * g + 3]}; }
    if (CH == 0) CX[w * 96 + 32 + lane] = c1[0];
    WAITL(); lds_arrive(lds, L_CNT + 52 + 4 * (w >> 1), lane); lds_wait(lds, L_CNT + 52 + 4 * (w >> 1), 2);
    const LAS float* ST = (const LAS float*)(lds + L_ST);
    const float mb = ST[32 * RT + r], rs = ST[64 + 32 * RT + r];
#pragma unroll
    for (int g = 0; g < 4; ++g) { const f32x4 o = *(const LAS f32x4*)(CX + (w ^ 1) * 96 + 8 * g + 4 * hh);
        for (int q = 0; q < 4; ++q) a0[4 * g + q] -= mb * (c0[4 * g + q] + o[q]); }
    if (CH == 0) { const float cv = c1[0] + CX[(w ^ 1) * 96 + 32 + lane];
#pragma unroll
        for (int g = 0; g < 4; ++g) { const f32x4 m4 = *(const LAS f32x4*)(ST + 32 * RT + 8 * g + 4 * hh); for (int q = 0; q < 4; ++q) a1[4 * g + q] -= m4[q] * cv; } }
    jobf = __builtin_bit_cast(u32x4, tr_frag((const LAS char*)lds + L_RING + w * 1024 + (4 * hh + ((lane & 15) >> 2)) * 64 + 32 * gp + 8 * (lane & 3), 0, 512));
    asm volatile("" : "+v"(a0), "+v"(a1), "+v"(jobf) :: "memory"); WAITL(); lds_arrive(lds, L_CNT + 68, lane);
    const int rtg = 2 * i + RT, h = 4 * j + 2 * cg + CH;
    { u32x4 k4;
      for (int q = 0; q < 4; ++q) k4[q] = cvtpk(a0[8 + 2 * q] * rs, a0[8 + 2 * q + 1] * rs);
      const int pr = (r & ~12) | ((r & 4) << 1) | ((r & 8) >> 1);
      st_sc1(wsr, OFF_KF + ((h * 64 + rtg) * 64 + pr + 32 * hh) * 16, k4); }
    if (CH == 0) {
        float v[16];
#pragma unroll
        for (int g = 0; g < 4; ++g) { const f32x4 a = *(const LAS f32x4*)(ST + 64 + 32 * RT + 8 * g + 4 * hh);
            for (int q = 0; q < 4; ++q) v[4 * g + q] = a1[4 * g + q] * a[q]; }
        const unsigned vb = OFF_VP + ((4 * j + 2 * cg + gp) * 64 + rtg) * 1024 + hh * 256 + (lane & 15) * 16;
#pragma unroll
        for (int s = 0; s < 2; ++s) { u32x4 o; for (int q = 0; q < 4; ++q) o[q] = cvtpk(v[8 * s + 2 * q], v[8 * s + 2 * q + 1]); st_sc1(wsr, vb + s * 512, o); }
    }
    { u32x4 q4; const float qs = rs * QSCALE;
      for (int q = 0; q < 4; ++q) q4[q] = cvtpk(a0[2 * q] * qs, a0[2 * q + 1] * qs);
      st_sc1(wsr, OFF_QF + ((h * 64 + rtg) * 64 + lane) * 16, q4); }
}

DI void p1_g(const KP& p, char* lds, int i, int j, int g, int lane, u64& tag TSARG) {
    const unsigned lds0 = (unsigned)(uintptr_t)lds;
    const int hh = lane >> 5, gp = (lane >> 4) & 1;
    const int drow = 8 * g + (lane >> 3);
    const unsigned xoff = (64 * i + drow) * 1024 + 16 * ((lane & 7) ^ ((drow >> 1) & 7));
    const unsigned wsw = (lane & 7) ^ (((drow >> 1) & 1) << 2);
    const unsigned woff0 = drow * 3072 + j * 128 + 16 * wsw, woff1 = drow * 3072 + 1024 + j * 128 + 16 * (wsw ^ 2);
    const int rot = (i + 3 * j) & 7;
    auto issue = [&](int c) { const unsigned st = lds0 + (c & 3) * P1_STAGE + g * 1024; const int kc = (c + rot) & 7;
        const char* xb = p.x + kc * 128; const char* wb = p.wqkv + (size_t)kc * (64 * 3072);
#pragma unroll
        for (int a = 0; a < 2; ++a) {
            glds16(xb + a * 32768, xoff, st + a * 4096);
            glds16(wb + a * 98304, woff0, st + P1_XB + a * 4096); glds16(wb + a * 98304, woff1, st + P1_XB + 8192 + a * 4096); glds16(wb + a * 98304 + 2048, woff0, st + P1_XB + 16384 + a * 4096); } };
    issue(0); issue(1); issue(2); issue(3);
    WAITV(24);
    STAMPK(2);
    BAR();
    { const u64 e = *(const LAS u64*)(lds + L_CNT + 32); tag = (u64)rfl((unsigned)e) | ((u64)rfl((unsigned)(e >> 32)) << 32); }
    const int f0 = blockIdx.x * 8 + g;
#pragma unroll
    for (int a = 0; a < 2; ++a) { int f = f0 + 4 * a; f = f < 1888 ? f : 1887; const bool is2 = f < 1376; const int ff = is2 ? f : f - 1376;
        glds16(is2 ? p.w2 : p.wo, ((ff >> 4) * 16 + (lane >> 2)) * 1024 + (ff & 15) * 64 + (lane & 3) * 16, lds0 + L_RING + (g + 4 * a) * 1024); }
    WAITV(18); BAR(); issue(4);
    WAITV(18); BAR(); STAMPK(1); issue(5); STAMPK(9);
    WAITV(16); STAMPK(10); BAR(); STAMPK(11); issue(6);
    WAITV(16); STAMP2(8 + g, 3); BAR(); issue(7);
    const unsigned so = (64 * i + lane) * 16;
    if (g == 0) glds16_sc1(p.ws + OFF_ST, so, lds0 + L_CNT + 1024);
    const int c8 = blockIdx.x & 7, t8 = blockIdx.x >> 3, rtf = 4 * (8 * (c8 & 1) + (t8 & 7)) + g;
    asm volatile("s_mov_b32 m0, %2\n\ts_nop 0\n\tglobal_load_lds_dword %0, %1 sc1" :: "v"((unsigned)((rtf * 32 + (lane & 31)) * 64)), "s"(p.ws + OFF_FX), "s"(lds0 + L_RING + 20480 + g * 256) : "memory");
    WAITV(17); BAR();
    WAITV(9); BAR();
    l2_touch(p, lds, g, lane); l2_touch(p, lds, g + 4, lane);
    WAITV(2);
    { const unsigned fl = *(const LAS unsigned*)(lds + L_RING + 20480 + g * 256 + lane * 4);
      if (__ballot(fl != (unsigned)tag) == 0ull) {
          const unsigned o = OFF_XF + rtf * 32768 + ((blockIdx.x >> 6) * 128 + lane) * 64;
          asm volatile("s_mov_b32 m0, %3\n\ts_nop 0\n\tglobal_load_lds_dword %0, %2 sc1\n\tglobal_load_lds_dword %1, %2 sc1" :: "v"(o), "v"(o + 4096), "s"(p.ws), "s"(lds0 + L_CNT + 256) : "memory"); } }
    if (g == 0) {
        u32x4 rec = *(const LAS u32x4*)(lds + L_CNT + 1024 + lane * 16);
        for (unsigned spins = 0; spins < SPIN_CAP && __ballot(rec[1] != (unsigned)tag || rec[3] != (unsigned)tag) != 0ull; ++spins)
            rec = ld_sc1(mkrsrc(p.ws + OFF_ST, 32768), so);
        LAS unsigned* ST = (LAS unsigned*)(lds + L_ST); ST[lane] = rec[0]; ST[64 + lane] = rec[2];
    }
    WAITL(); STAMP2(8 + g, 6); BAR();
}

template <int NP, int NW>
DI void g_ffn1(const KP& p, char* lds, int i, int j, int P0, int g, int lane, u64 tag TSARG) {
    const unsigned lds0 = (unsigned)(uintptr_t)lds;
    const int hh = lane >> 5, gp = (lane >> 4) & 1;
    const int rtg = 4 * i + g;
    const int s0 = g, s1 = g + 4;
    const bool t0 = s0 >= NP;
    const char* b0 = t0 ? p.w3 : p.w1; const char* b1 = p.w3;
    const unsigned d0 = (lane >> 2) * (FF * 2) + (P0 + (t0 ? s0 - NP : s0)) * 64 + 16 * ((lane & 3) ^ (t0 ? 2 : 0));
    const unsigned d1 = (lane >> 2) * (FF * 2) + (P0 + s1 - NP) * 64 + 16 * ((lane & 3) ^ 2);
    const unsigned m0a = lds0 + L_RING + s0 * 1024, m0b = lds0 + L_RING + s1 * 1024;
    const unsigned rowp = lds0 + L_RING + gp * NP * 1024 + (8 * hh + ((lane & 15) >> 2)) * 64 + 8 * (lane & 3);
    const unsigned wa0 = rowp + 32 * gp, wa1 = rowp + 32 * (gp ^ 1);
    const char* xb = p.ws + OFF_XF + (size_t)rtg * 32768; const unsigned xo = lane * 16, cntw = lds0 + L_CNT + 4 * g, cntr = lds0 + L_CNT + 4 * (lane & 3);
    flag_wait(p.ws + OFF_FX + (rtg * 32 + (lane & 31)) * 64, tag, 1);
    lds_wait(lds, L_CNT + 68, 8);
    STAMPK(3);
    f32x16 a0 = {}, a1 = {}, a2 = {}, a3 = {}, a4 = {}, a5 = {};
#define PROFOP
    if constexpr (NP == 3) {
        if constexpr (NW == 2) asm volatile(FFN1_BODY_3_2 : [a0] "+v"(a0), [a1] "+v"(a1), [a2] "+v"(a2), [a3] "+v"(a3), [a4] "+v"(a4), [a5] "+v"(a5) PROFOP
            : [b0] "s"(b0), [b1] "s"(b1), [xb] "s"(xb), [d0] "v"(d0), [d1] "v"(d1), [m0a] "s"(m0a), [m0b] "s"(m0b), [wa0] "v"(wa0), [wa1] "v"(wa1), [xo] "v"(xo), [cntw] "v"(cntw), [cntr] "v"(cntr) : FFN1_CLOB, "scc", "memory");
        else asm volatile(FFN1_BODY_3_1 : [a0] "+v"(a0), [a1] "+v"(a1), [a2] "+v"(a2), [a3] "+v"(a3), [a4] "+v"(a4), [a5] "+v"(a5) PROFOP
            : [b0] "s"(b0), [xb] "s"(xb), [d0] "v"(d0), [m0a] "s"(m0a), [wa0] "v"(wa0), [wa1] "v"(wa1), [xo] "v"(xo), [cntw] "v"(cntw), [cntr] "v"(cntr) : FFN1_CLOB, "scc", "memory");
    } else asm volatile(FFN1_BODY_2_1 : [a0] "+v"(a0), [a1] "+v"(a1), [a2] "+v"(a2), [a3] "+v"(a3) PROFOP
            : [b0] "s"(b0), [xb] "s"(xb), [d0] "v"(d0), [m0a] "s"(m0a), [wa0] "v"(wa0), [wa1] "v"(wa1), [xo] "v"(xo), [cntw] "v"(cntw), [cntr] "v"(cntr) : FFN1_CLOB, "scc", "memory");
    STAMPK(4);
    lane = lane_now();
    const __amdgpu_buffer_rsrc_t hr = mkrsrc(p.ws + OFF_HF, 64 * 86 * 1024);
    auto epi = [&](const f32x16& a, int u) { float hv[8];
        for (int q = 0; q < 8; ++q) { const float gg = a[q], e = __builtin_amdgcn_exp2f(fmulc_s(gg, -1.4426950408889634f)); hv[q] = fmul_t(fmul_s(gg, a[8 + q]), __builtin_amdgcn_rcpf(fadd1_t(e))); }
        u32x4 o; for (int q = 0; q < 4; ++q) o[q] = cvtpk(hv[2 * q], hv[2 * q + 1]);
        st_sc1(hr, (rtg * 86 + 2 * P0 + u) * 1024 + lane * 16, o); };
    epi(a0, 0); epi(a1, 1); epi(a2, 2); epi(a3, 3);
    if constexpr (NP == 3) { epi(a4, 4); epi(a5, 5); }
    flag_wait(p.ws + OFF_FS + (g * 64 + lane) * 64, tag, 2);
    WAITV(0);
    if (lane == 0) flag_set(p.ws + OFF_F2 + (rtg * 16 + j) * 64, tag);
}

__global__ void __launch_bounds__(768, 3) fused_block(KP p) {
    extern __shared__ __attribute__((aligned(16))) char lds[];
    const int b = blockIdx.x, c = b & 7, t = b >> 3;
    const int lane = threadIdx.x & 63, w = rfl(threadIdx.x >> 6);
    const int r = lane & 31, hh = lane >> 5;
    const unsigned lds0 = (unsigned)(uintptr_t)lds;
    const int i1 = 16 * (c & 1) + (t & 15), j1 = 2 * (c >> 1) + (t >> 4);
    const int rt64 = 4 * c + (t & 3), ct64 = t >> 2;
    TSDECL; STAMPK(0);
    u32x4 jobf = {};
    u64 tag;
    if (threadIdx.x < 26) ((LAS unsigned*)(lds + L_CNT))[threadIdx.x < 8 ? threadIdx.x : threadIdx.x + 2] = threadIdx.x == 10 ? 8u : (threadIdx.x >= 16 && threadIdx.x < 24) ? 16u : 0u;
    if (w < 8) {
        if (CODE_SPAN > 0) {
          u64 pc;
          asm volatile("s_getpc_b64 s[60:61]\n\ts_add_u32 s60, s60, _Z11fused_block2KP@rel32@lo+4\n\ts_addc_u32 s61, s61, _Z11fused_block2KP@rel32@hi+12\n\ts_mov_b64 %0, s[60:61]" : "=s"(pc) :: "s60", "s61", "scc");
          const unsigned o = (w * 64 + lane) * 64, dst = lds0 + L_CNT + 256;
          asm volatile("s_mov_b32 m0, %2\n\ts_nop 0\n\tglobal_load_lds_dword %0, %1" :: "v"(o), "s"(pc), "s"(dst) : "memory");
          if (w < 2) asm volatile("s_mov_b32 m0, %2\n\ts_nop 0\n\tglobal_load_lds_dword %0, %1" :: "v"(min(o + 32768, (unsigned)(CODE_SPAN - 64))), "s"(pc), "s"(dst) : "memory"); }
        const int lb = 128 * (c & 1) + 32 * (c >> 1) + t;
        const u32x4 xr = *(const u32x4*)(p.x + (lb * 8 + w) * 1024 + lane * 16);
        asm volatile("" ::: "memory");
        { const u64 e = __hip_atomic_load((const gu64*)(p.ws + OFF_EP), __ATOMIC_RELAXED, __HIP_MEMORY_SCOPE_AGENT) + 1ull; tag = (u64)rfl((unsigned)e) | ((u64)rfl((unsigned)(e >> 32)) << 32); }
        if (threadIdx.x == 0) *(LAS u64*)(lds + L_CNT + 32) = tag;
        if (w < 4) p1_a<0>(p, lds, i1, j1, w, lane, tag, xr, lb, jobf TSPASS); else p1_a<1>(p, lds, i1, j1, w, lane, tag, xr, lb, jobf TSPASS);
    } else p1_g(p, lds, i1, j1, w - 8, lane, tag TSPASS);
    if (w < 8) { WAITV(0); WAITL(); }
    lds_arrive(lds, L_CNT + 28, lane);
    STAMPK(5);
    if (w < 8) {
        const int lane = lane_now(), r = lane & 31, hh = lane >> 5;
        const int h = 4 * c + (t & 3), rb = t >> 2, qt = rb * 8 + w;
        const char* kb = p.ws + OFF_KF + (size_t)h * 65536; const char* vb = p.ws + OFF_VP + (size_t)h * 65536;
        const __amdgpu_buffer_rsrc_t wsr = mkrsrc(p.ws, 10u << 20);
        const int jp = h >> 2;
        auto flag_of = [&](int ip) { return p.ws + OFF_F1 + ((ip >> 4) + 2 * (jp >> 1) + 8 * ((ip & 15) + 16 * (jp & 1))) * 64; };
        if (w == 0) { lds_wait(lds, L_CNT + 28, 12); if (lane == 0) flag_set(p.ws + OFF_F1 + b * 64, tag);
            const int q = lane & 15; flag_wait(flag_of(q < 12 ? q : 4 * rb + (q & 3)), tag, 3); lds_arrive(lds, L_CNT + 40, lane); }
        else lds_wait(lds, L_CNT + 40, 1);
        STAMPK(6);
#pragma unroll
        for (int blk = 0; blk < 3; ++blk) { const unsigned off = (blk * 8 + w) * 1024;
            glds16_sc1(kb, off + lane * 16, lds0 + L_K + off); glds16_sc1(vb, off + lane * 16, lds0 + L_V + off); }
        const bf16x8 q = asb(ld_sc1(wsr, OFF_QF + ((h * 64 + qt) * 64 + lane) * 16));
        WAITV(0);
        lds_arrive(lds, L_CNT + 16, lane);
        { const u32x4 o = {~0u, ~0u, ~0u, ~0u}; st_sc1(wsr, OFF_OF + ((b * 8 + w) * 64 + lane) * 16, o); }
        { const int f = b * 8 + w; if (f < 1888) st_sc1(mkrsrc(p.ws + OFF_W2F, 2u << 20), (f < 1376 ? f * 1024 : (int)(OFF_WOF - OFF_W2F) + (f - 1376) * 1024) + lane * 16, jobf); }
        if (w == 0) { flag_wait(flag_of(12 + (lane & 31) % 20), tag, 4); lds_arrive(lds, L_CNT + 40, lane); }
        lds_wait(lds, L_CNT + 16, 8);
        STAMPK(7);
        const LAS char* kl = (const LAS char*)lds + L_K + lane * 16;
        float mx;
        { bf16x8 k0 = *(const LAS bf16x8*)(kl), k1 = *(const LAS bf16x8*)(kl + 1024);
          f32x16 z = {}; f32x16 s0 = MFMA(k0, q, z), s1 = MFMA(k1, q, z);
          mx = rowmax16(s0, -INFINITY); mx = rowmax16(s1, mx); mx = xhalf_max(mx); }
        float negm = -(mx + 64.0f);
        f32x16 o; float l;
        const u64 msk = 0x0000FFFF0000FFFFull;
        for (int attempt = 0; attempt < 2; ++attempt) {
            o = f32x16{};
            unsigned ka = lds0 + L_K + lane * 16, va = lds0 + L_V + hh * 256 + (lane & 15) * 16; int n = 8;
            const unsigned gcnt = lds0 + L_CNT + 44 + 4 * attempt, fso = OFF_FS + b * 64; const unsigned pub = rfl((w == 0 && attempt == 0) ? 1u : 0u);
            const unsigned young = rfl(w >> 2), pw = lds0 + L_CNT + 72 + 4 * w, pr = lds0 + L_CNT + 72 + 4 * (w ^ 4), nb = rfl(attempt == 0 ? 7u : 99u), seen = lds0 + L_CNT + 40, ldsw = lds0 + L_K + w * 1024, vo = lane * 16; const char* kb3 = kb + (24 + w) * 1024; const char* vb3 = vb + (24 + w) * 1024;
            asm volatile(ATTN_BODY : [o] "+v"(o), [ka] "+v"(ka), [va] "+v"(va), [n] "+s"(n) : [q] "v"(q), [negm] "v"(negm), [msk] "s"(msk), [cnt] "v"(gcnt), [pub] "s"(pub), [fso] "v"(fso), [tag] "v"(tag), [ws] "s"(p.ws), \
                [young] "s"(young), [pw] "v"(pw), [pr] "v"(pr), [nb] "s"(nb), [seen] "v"(seen), [kb] "s"(kb3), [vb] "s"(vb3), [ldsw] "s"(ldsw), [vo] "v"(vo) : ATTN_CLOB, "scc", "memory");
            l = o[8];
            bool ok = (l >= 0x1p-72f) && (l < 0x1p126f);
            for (int k = 0; k < 8; ++k) ok = ok && (__builtin_fabsf(o[k]) < INFINITY);
            if (__ballot(!ok) == 0ull || attempt == 1) break;
            float m = -INFINITY;
            for (int k = 0; k < 64; ++k) { bf16x8 k0 = *(const LAS bf16x8*)(kl + k * 1024); f32x16 z = {}; f32x16 s0 = MFMA(k0, q, z); m = rowmax16(s0, m); }
            negm = -xhalf_max(m);
        }
        STAMPK(8);
        lds_wait(lds, L_CNT + 20, 1);
        { const float rl = 1.0f / l; u32x4 pk;
          for (int k = 0; k < 4; ++k) pk[k] = min(cvtpk(o[2 * k] * rl, o[2 * k + 1] * rl), 0xFFFFFFFEu);
          st_sc1(wsr, OFF_OF + (qt * 32 + h) * 1024 + lane * 16, pk); }
        const int a = w & 1, kq = w >> 1;
        const __amdgpu_buffer_rsrc_t wr = mkrsrc(p.ws + OFF_WOF, 512 * 1024);
        const unsigned l16 = lane * 16;
        u32x4 wf[16];
#pragma unroll
        for (int k = 0; k < 16; ++k) wf[k] = ld_sc1s(wr, l16, rfl(((8 * kq + (k >> 1)) * 16 + 2 * ct64 + (k & 1)) * 1024));
        const int bb = kq & 1, hf = kq >> 1;
        const int row = 64 * rt64 + 32 * a + r, col0 = 64 * ct64 + 32 * bb + 16 * hf + 4 * hh;
        u32x2 xr[2];
#pragma unroll
        for (int g = 0; g < 2; ++g) xr[g] = *(const u32x2*)(p.x + row * 1024 + (col0 + 8 * g) * 2);
        lds_arrive(lds, L_CNT + 104, lane);
        STAMPK(9);
        const unsigned obase = OFF_OF + ((2 * rt64 + a) * 32 + 8 * kq) * 1024;
        for (unsigned spins = 0; spins < SPIN_CAP; ++spins) {
            asm volatile("" ::: "memory");
            const unsigned v = __builtin_amdgcn_raw_buffer_load_b32(wsr, obase + (lane & 7) * 1024 + 1020, 0, 16);
            if (__ballot(v == 0xFFFFFFFFu) == 0ull) break;
            __builtin_amdgcn_s_sleep(2); }
        STAMPK(10);
        f32x16 s0, s1;
        for (unsigned spins = 0; spins < SPIN_CAP; ++spins) {
            asm volatile("" ::: "memory");
            u32x4 of[8];
#pragma unroll
            for (int k = 0; k < 8; ++k) of[k] = ld_sc1s(wsr, l16, rfl(obase + k * 1024));
            s0 = f32x16{}; s1 = f32x16{}; unsigned m = 0;
#pragma unroll
            for (int k = 0; k < 8; ++k) { s0 = MFMA(asb(wf[2 * k]), asb(of[k]), s0); s1 = MFMA(asb(wf[2 * k + 1]), asb(of[k]), s1); m = max(max(m, max(of[k][0], of[k][1])), max(of[k][2], of[k][3])); }
            if (__ballot(m == 0xFFFFFFFFu) == 0ull) break;
            __builtin_amdgcn_s_sleep(2); }
        if (b == 0 && threadIdx.x == 0) flag_set(p.ws + OFF_EP, tag);
        LAS f32x4* X = (LAS f32x4*)lds;
        lds_wait(lds, L_CNT + 104, 8);
#pragma unroll
        for (int d = 0; d < 4; ++d) if (d != kq) {
#pragma unroll
            for (int q = 0; q < 2; ++q) { const int g = 2 * (d >> 1) + q; const f32x16& s = (d & 1) ? s1 : s0;
                X[4096 + ((a * 4 + d) * 4 + kq) * 128 + q * 64 + lane] = f32x4{s[4 * g], s[4 * g + 1], s[4 * g + 2], s[4 * g + 3]}; } }
        WAITL(); lds_arrive(lds, L_CNT + 108, lane); lds_wait(lds, L_CNT + 108, 12);
        STAMPK(11);
        { const int tau = 2 * a + bb;
#pragma unroll
          for (int q = 0; q < 2; ++q) { const int g = 2 * hf + q;
            f32x4 sv; { const f32x16& s = bb ? s1 : s0; const int gg = hf ? 2 + q : q; sv = hf ? (bb ? f32x4{s1[8 + 4 * q], s1[9 + 4 * q], s1[10 + 4 * q], s1[11 + 4 * q]} : f32x4{s0[8 + 4 * q], s0[9 + 4 * q], s0[10 + 4 * q], s0[11 + 4 * q]})
                                                                                                    : (bb ? f32x4{s1[4 * q], s1[1 + 4 * q], s1[2 + 4 * q], s1[3 + 4 * q]} : f32x4{s0[4 * q], s0[1 + 4 * q], s0[2 + 4 * q], s0[3 + 4 * q]}); (void)s; (void)gg; }
#pragma unroll
            for (int src = 1; src < 4; ++src) sv += X[4096 + ((a * 4 + kq) * 4 + ((kq + src) & 3)) * 128 + q * 64 + lane];
            const f32x4 f = (X[((0 * 4 + tau) * 4 + g) * 64 + lane] + X[((1 * 4 + tau) * 4 + g) * 64 + lane]) + (X[((2 * 4 + tau) * 4 + g) * 64 + lane] + X[((3 * 4 + tau) * 4 + g) * 64 + lane]);
            float y[4];
            for (int q4 = 0; q4 < 4; ++q4) { const float ff = f[q4], gate = ff * __builtin_amdgcn_rcpf(1.0f + __builtin_amdgcn_exp2f(ff * -1.4426950408889634f));
                const unsigned xw = xr[q][q4 >> 1]; const float xv = __uint_as_float((q4 & 1) ? (xw & 0xFFFF0000u) : (xw << 16));
                y[q4] = xv + sv[q4] * gate; }
            u32x2 ov = {cvtpk(y[0], y[1]), cvtpk(y[2], y[3])};
            *(u32x2*)(p.out + row * 1024 + (col0 + 8 * q) * 2) = ov; } }
        STAMPK(12); STAMP_FLUSH();
    } else {
        const int g = w - 8;
        __builtin_amdgcn_s_setprio(3);
        { const int i = 8 * (c & 1) + (t & 7), j = 4 * (c >> 1) + (t >> 3), P0 = j < 11 ? 3 * j : 33 + 2 * (j - 11);
          if (j < 11) { if (g < 2) g_ffn1<3, 2>(p, lds, i, j, P0, g, lane, tag TSPASS); else g_ffn1<3, 1>(p, lds, i, j, P0, g, lane, tag TSPASS); }
          else g_ffn1<2, 1>(p, lds, i, j, P0, g, lane, tag TSPASS); }
        STAMPK(6);
        const int lane = lane_now();
        lds_arrive(lds, L_CNT + 24, lane);
        if (g == 0) { lds_wait(lds, L_CNT + 24, 4); lds_arrive(lds, L_CNT + 20, lane);
            flag_wait(p.ws + OFF_F2 + ((2 * rt64 + ((lane >> 4) & 1)) * 16 + (lane & 15)) * 64, tag, 5); lds_arrive(lds, L_CNT + 20, lane); }
        else {
            lds_wait(lds, L_CNT + 24, 4);
            for (int k = 0; k < 4; ++k) { const unsigned s = ((g - 1) * 4 + k) * 64 + lane;
                const unsigned u = (t & 3) * 22 + (s >> 5), o = (u * 16 + 2 * ct64) * 1024 + (s & 31) * 64;
                if (s < 704 && u < 86) asm volatile("s_mov_b32 m0, %2\n\ts_nop 0\n\tglobal_load_lds_dword %0, %1 sc1" :: "v"(o), "s"(p.ws + OFF_W2F), "s"(lds0 + L_CNT + 256) : "memory"); }
            lds_wait(lds, L_CNT + 20, 2); }
        for (int k = 0; k < 2; ++k) { const unsigned s = (g * 2 + k) * 64 + lane, sec = ct64 * 344 + s;
            if (s < 344) asm volatile("s_mov_b32 m0, %2\n\ts_nop 0\n\tglobal_load_lds_dword %0, %1 sc1" :: "v"(2 * rt64 * 86 * 1024 + sec * 64), "s"(p.ws + OFF_HF), "s"(lds0 + L_CNT + 256) : "memory"); }
        STAMPK(7);
        const __amdgpu_buffer_rsrc_t hr = mkrsrc(p.ws + OFF_HF, 64 * 86 * 1024), w2 = mkrsrc(p.ws + OFF_W2F, 1376 * 1024);
        const int u0 = g < 2 ? 22 * g : 44 + 21 * (g - 2), ul = u0 + (g < 2 ? 21 : 20);
        f32x16 f00 = {}, f01 = {}, f10 = {}, f11 = {};
        u32x4 h0[4], h1[4], wA[4], wB[4];
        const unsigned l16 = lane * 16;
        auto load = [&](int st, int u) { u = u < ul ? u : ul;
            const unsigned ho = rfl(((2 * rt64) * 86 + u) * 1024), wo = rfl((u * 16 + 2 * ct64) * 1024);
            h0[st] = ld_sc1s(hr, l16, ho); h1[st] = ld_sc1s(hr, l16, ho + 86 * 1024);
            wA[st] = ld_sc1s(w2, l16, wo); wB[st] = ld_sc1s(w2, l16, wo + 1024); };
        auto mma = [&](int st) { f00 = MFMA(asb(wA[st]), asb(h0[st]), f00); f01 = MFMA(asb(wB[st]), asb(h0[st]), f01); f10 = MFMA(asb(wA[st]), asb(h1[st]), f10); f11 = MFMA(asb(wB[st]), asb(h1[st]), f11); };
#pragma unroll
        for (int st = 0; st < 4; ++st) load(st, u0 + st);
#define FFN2_NOP 7
#define STR2(x) #x
#define STR(x) STR2(x)
#define FFN2_GAP "s_nop " STR(FFN2_NOP)
#define SB0 __builtin_amdgcn_sched_barrier(0)
#pragma unroll 1
        for (int k = 0; k < 20; k += 4) {
#pragma unroll
            for (int st = 0; st < 4; ++st) {
                int u = u0 + k + st + 4; u = u < ul ? u : ul;
                const unsigned ho = rfl(((2 * rt64) * 86 + u) * 1024), wo = rfl((u * 16 + 2 * ct64) * 1024);
                f00 = MFMA(asb(wA[st]), asb(h0[st]), f00); SB0; asm volatile(FFN2_GAP); SB0;
                f01 = MFMA(asb(wB[st]), asb(h0[st]), f01); SB0; h0[st] = ld_sc1s(hr, l16, ho); asm volatile(FFN2_GAP); SB0;
                f10 = MFMA(asb(wA[st]), asb(h1[st]), f10); SB0; wA[st] = ld_sc1s(w2, l16, wo); asm volatile(FFN2_GAP); SB0;
                f11 = MFMA(asb(wB[st]), asb(h1[st]), f11); SB0; wB[st] = ld_sc1s(w2, l16, wo + 1024); h1[st] = ld_sc1s(hr, l16, ho + 86 * 1024); SB0; } }
        mma(0); if (g < 2) mma(1);
        __builtin_amdgcn_s_setprio(0);
        STAMPK(8);
        lds_wait(lds, L_CNT + 104, 8);
        LAS f32x4* X = (LAS f32x4*)lds;
#pragma unroll
        for (int q = 0; q < 4; ++q) {
            X[((g * 4 + 0) * 4 + q) * 64 + lane] = f32x4{f00[4 * q], f00[4 * q + 1], f00[4 * q + 2], f00[4 * q + 3]};
            X[((g * 4 + 1) * 4 + q) * 64 + lane] = f32x4{f01[4 * q], f01[4 * q + 1], f01[4 * q + 2], f01[4 * q + 3]};
            X[((g * 4 + 2) * 4 + q) * 64 + lane] = f32x4{f10[4 * q], f10[4 * q + 1], f10[4 * q + 2], f10[4 * q + 3]};
            X[((g * 4 + 3) * 4 + q) * 64 + lane] = f32x4{f11[4 * q], f11[4 * q + 1], f11[4 * q + 2], f11[4 * q + 3]}; }
        WAITL(); lds_arrive(lds, L_CNT + 108, lane);
        STAMPK(12); STAMP_FLUSH();
    }
}

extern "C" void kernel_launch(void* const* d_in, const int* in_sizes, int n_in, void* d_out, int out_size, void* d_ws, size_t ws_size, hipStream_t stream) {
    (void)in_sizes; (void)n_in; (void)out_size; (void)ws_size;
    (void)hipFuncSetAttribute((const void*)fused_block, hipFuncAttributeMaxDynamicSharedMemorySize, LDS_BYTES);
    KP p; p.x = (const char*)d_in[0]; p.wqkv = (const char*)d_in[1]; p.wo = (const char*)d_in[2]; p.w1 = (const char*)d_in[4]; p.w3 = (const char*)d_in[5]; p.w2 = (const char*)d_in[6];
    p.ws = (char*)d_ws; p.out = (char*)d_out;
    fused_block<<<256, 768, LDS_BYTES, stream>>>(p);
}
```
